```python
import torch

def _softmax(x, axis=-1):
    return torch.softmax(x, dim=axis)

def _gelu(x):
    return torch.nn.functional.gelu(x, approximate='tanh')

def _silu(x):
    return torch.nn.functional.silu(x)

def _sigmoid(x):
    return torch.sigmoid(x)

def _layernorm(x, axis=-1, eps=1e-05):
    m = x.mean(dim=axis, keepdim=True)
    v = x.var(dim=axis, keepdim=True, unbiased=False)
    return (x - m) / torch.sqrt(v + eps)

def _scatter_add(tgt, idx, src, axis=0):
    return tgt.clone().index_add_(axis, idx, src)

def _segment_sum(src, seg, n):
    out = torch.zeros((n,) + src.shape[1:], dtype=src.dtype)
    return out.index_add_(0, seg, src)

def _pad(x, pw):
    flat = tuple((v for pair in reversed(pw) for v in pair))
    return torch.nn.functional.pad(x, flat)
kernel_name = 'adaLN_cond__sliding_block_cached__swiglu_ffn__decode'

def setup_inputs():

    _w = lambda a: (lambda t: t.to(torch.bfloat16) if t.is_floating_point() else t)(torch.as_tensor(a))

    def _A_setup():
        B, Sq, D = (8, 1, 1024)
        Hq, Hkv, Sk, Dh = (32, 4, 4096, 128)
        assert Hq % Hkv == 0, (Hq, Hkv)
        return {'x': _w((lambda *s: torch.randn(*s, dtype=torch.bfloat16) * 0.05)(B, Sq, D)), 'k_cache': _w((lambda *s: torch.randn(*s, dtype=torch.bfloat16) * 3.2)(B, Hkv, Sk, Dh)), 'v_cache': _w((lambda *s: torch.randn(*s, dtype=torch.bfloat16) * 0.4)(B, Hkv, Sk, Dh)), 'wq': _w((lambda *s: torch.randn(*s, dtype=torch.bfloat16) * 3.2)(D, Hq * Dh) * D ** (-0.5)), 'wo': _w((lambda *s: torch.randn(*s, dtype=torch.bfloat16) * 0.4)(Hq * Dh, D) * (Hq * Dh) ** (-0.5))}
    _A_in = _A_setup()

    def _B_setup():
        B, S, D, F = (8, 1, 1024, 3584)
        return {'x': _w((lambda *s: torch.randn(*s, dtype=torch.bfloat16) * 0.05)(B, S, D)), 'w_gate': _w((lambda *s: torch.randn(*s, dtype=torch.bfloat16) * 0.05)(D, F)), 'w_up': _w((lambda *s: torch.randn(*s, dtype=torch.bfloat16) * 0.05)(D, F)), 'w_down': _w((lambda *s: torch.randn(*s, dtype=torch.bfloat16) * 0.05)(F, D))}
    _B_in = _B_setup()
    return {'A_x': _A_in['x'], 'A_k_cache': _A_in['k_cache'], 'A_v_cache': _A_in['v_cache'], 'A_wq': _A_in['wq'], 'A_wo': _A_in['wo'], 'B_x': _B_in['x'], 'B_w_gate': _B_in['w_gate'], 'B_w_up': _B_in['w_up'], 'B_w_down': _B_in['w_down'], 'c': _w((lambda *s: torch.randn(*s, dtype=torch.bfloat16) * 0.1)(8, 1024) * 8.0), 'w_mod': _w((lambda *s: torch.randn(*s, dtype=torch.bfloat16) * 0.1)(1024, 6 * 1024) * 1024 ** (-0.5) * 8.0)}

def reference(A_x, A_k_cache, A_v_cache, A_wq, A_wo, B_x, B_w_gate, B_w_up, B_w_down, c, w_mod):

    def _A_ref(x, k_cache, v_cache, wq, wo):
        B, Sq, D = x.shape
        Hq, Dh = (32, 128)
        Hkv, Sk = (k_cache.shape[1], k_cache.shape[2])
        W = min(4096, max(256, Sk // 4))
        rep = Hq // Hkv
        q = (lambda x, *d: x.permute(*d))((x @ wq).reshape(B, Sq, Hq, Dh), 0, 2, 1, 3)
        k = (lambda x, n, axis: torch.repeat_interleave(x, n, dim=axis))(k_cache[:, :, Sk - W:, :], rep, axis=1)
        v = (lambda x, n, axis: torch.repeat_interleave(x, n, dim=axis))(v_cache[:, :, Sk - W:, :], rep, axis=1)
        scores = torch.einsum('bhqd,bhkd->bhqk', q, k) * Dh ** (-0.5)
        p = _softmax(scores, axis=-1)
        o = torch.einsum('bhqk,bhkd->bhqd', p, v)
        return (lambda x, *d: x.permute(*d))(o, 0, 2, 1, 3).reshape(B, Sq, Hq * Dh) @ wo

    def _B_ref(x, w_gate, w_up, w_down):
        g = _silu(x @ w_gate)
        u = x @ w_up
        return g * u @ w_down
    x = A_x
    mod = (_silu(c) @ w_mod).reshape(c.shape[0], 6, 1, -1)
    sa, ca, ga, sb, cb, gb = (mod[:, 0].clone(), mod[:, 1].clone(), mod[:, 2].clone(), mod[:, 3].clone(), mod[:, 4].clone(), mod[:, 5].clone())
    h = _layernorm(x, axis=-1) * (1 + ca) + sa
    x = x + ga * _A_ref(h, A_k_cache, A_v_cache, A_wq, A_wo)
    h = _layernorm(x, axis=-1) * (1 + cb) + sb
    x = x + gb * _B_ref(h, B_w_gate, B_w_up, B_w_down)
    return x
```

```cpp
#include <hip/hip_runtime.h>
#include <hip/hip_bf16.h>
#include <stdint.h>

typedef unsigned short u16;
__device__ __forceinline__ float bf2f(u16 b) { return __uint_as_float(((unsigned)b) << 16); }
__device__ __forceinline__ u16 f2bf(float f) { unsigned u = __float_as_uint(f); u += 0x7FFFu + ((u >> 16) & 1u); return (u16)(u >> 16); }
__device__ __forceinline__ float rbf(float f) { return bf2f(f2bf(f)); }

__global__ void __launch_bounds__(256) skinny(const float* act, const u16* W, float* out, int K, int N) {
  __shared__ float red[4][8][64];
  __shared__ float sact[8 * 4096];
  int tid = threadIdx.x, lane = tid & 63, w = tid >> 6;
  for (int i = tid; i < 8 * K; i += 256) sact[i] = act[i];
  __syncthreads();
  int n = blockIdx.x * 64 + lane;
  float acc[8] = {0, 0, 0, 0, 0, 0, 0, 0};
  for (int k = w; k < K; k += 4) {
    float wv = bf2f(W[(size_t)k * N + n]);
#pragma unroll
    for (int b = 0; b < 8; ++b) acc[b] += sact[b * K + k] * wv;
  }
  for (int b = 0; b < 8; ++b) red[w][b][lane] = acc[b];
  __syncthreads();
  if (w == 0) for (int b = 0; b < 8; ++b) out[b * N + n] = rbf(red[0][b][lane] + red[1][b][lane] + red[2][b][lane] + red[3][b][lane]);
}

__global__ void k_silu_c(const u16* c, float* sc) {
  int i = blockIdx.x * 256 + threadIdx.x;
  float v = bf2f(c[i]);
  sc[i] = rbf(v / (1.f + __expf(-v)));
}

__global__ void __launch_bounds__(256) k_ln(const float* x, const float* mod, int soff, int coff, float* h) {
  __shared__ float red[256];
  int b = blockIdx.x, tid = threadIdx.x;
  float s = 0;
  for (int i = tid; i < 1024; i += 256) s += x[b * 1024 + i];
  red[tid] = s; __syncthreads();
  for (int o = 128; o > 0; o >>= 1) { if (tid < o) red[tid] += red[tid + o]; __syncthreads(); }
  float m = rbf(red[0] / 1024.f); __syncthreads();
  float mean_exact = red[0] / 1024.f;
  s = 0;
  for (int i = tid; i < 1024; i += 256) { float d = x[b * 1024 + i] - mean_exact; s += d * d; }
  __syncthreads();
  red[tid] = s; __syncthreads();
  for (int o = 128; o > 0; o >>= 1) { if (tid < o) red[tid] += red[tid + o]; __syncthreads(); }
  float v = rbf(red[0] / 1024.f);
  float den = rbf(sqrtf(rbf(v + 1e-5f)));
  for (int i = tid; i < 1024; i += 256) {
    float xn = rbf(rbf(x[b * 1024 + i] - m) / den);
    float c1 = rbf(1.f + mod[b * 6144 + coff + i]);
    h[b * 1024 + i] = rbf(rbf(xn * c1) + mod[b * 6144 + soff + i]);
  }
}

__global__ void k_cvt(const u16* x, float* o) { int i = blockIdx.x * 256 + threadIdx.x; o[i] = bf2f(x[i]); }

__global__ void __launch_bounds__(256) k_attn(const float* q, const u16* kc, const u16* vc, float* o) {
  __shared__ float sq[128];
  __shared__ float sc[1024];
  __shared__ float red[256];
  int b = blockIdx.x / 32, h = blockIdx.x % 32, g = h / 8, tid = threadIdx.x;
  if (tid < 128) sq[tid] = q[b * 4096 + h * 128 + tid];
  __syncthreads();
  const u16* kb = kc + ((size_t)(b * 4 + g) * 4096 + 3072) * 128;
  const u16* vb = vc + ((size_t)(b * 4 + g) * 4096 + 3072) * 128;
  float mx = -INFINITY;
  for (int j = tid; j < 1024; j += 256) {
    float s = 0;
    for (int d = 0; d < 128; ++d) s += sq[d] * bf2f(kb[j * 128 + d]);
    s = rbf(rbf(s) * 0.08838834764831845f);
    sc[j] = s; mx = fmaxf(mx, s);
  }
  red[tid] = mx; __syncthreads();
  for (int o2 = 128; o2 > 0; o2 >>= 1) { if (tid < o2) red[tid] = fmaxf(red[tid], red[tid + o2]); __syncthreads(); }
  mx = red[0]; __syncthreads();
  float s = 0;
  for (int j = tid; j < 1024; j += 256) { float e = __expf(sc[j] - mx); sc[j] = e; s += e; }
  red[tid] = s; __syncthreads();
  for (int o2 = 128; o2 > 0; o2 >>= 1) { if (tid < o2) red[tid] += red[tid + o2]; __syncthreads(); }
  float inv = 1.f / red[0];
  if (tid < 128) {
    float a = 0;
    for (int j = 0; j < 1024; ++j) a += rbf(sc[j] * inv) * bf2f(vb[j * 128 + tid]);
    o[b * 4096 + h * 128 + tid] = rbf(a);
  }
}

__global__ void k_res(const float* x, const float* mod, int goff, const float* y, float* out) {
  int i = blockIdx.x * 256 + threadIdx.x; int b = i / 1024, d = i % 1024;
  out[i] = rbf(x[i] + rbf(mod[b * 6144 + goff + d] * y[i]));
}
__global__ void k_act(const float* g, const float* u, float* a) {
  int i = blockIdx.x * 256 + threadIdx.x;
  float v = g[i];
  a[i] = rbf(rbf(v / (1.f + __expf(-v))) * u[i]);
}
__global__ void k_out(const float* x, __hip_bfloat16* o) { int i = blockIdx.x * 256 + threadIdx.x; ((u16*)o)[i] = f2bf(x[i]); }

extern "C" void kernel_launch(void* const* d_in, const int* in_sizes, int n_in, void* d_out, int out_size, void* d_ws, size_t ws_size, hipStream_t stream) {
  const u16* A_x = (const u16*)d_in[0]; const u16* kc = (const u16*)d_in[1]; const u16* vc = (const u16*)d_in[2];
  const u16* wq = (const u16*)d_in[3]; const u16* wo = (const u16*)d_in[4];
  const u16* wg = (const u16*)d_in[6]; const u16* wu = (const u16*)d_in[7]; const u16* wd = (const u16*)d_in[8];
  const u16* c = (const u16*)d_in[9]; const u16* wm = (const u16*)d_in[10];
  float* ws = (float*)d_ws;
  float* sc = ws; float* mod = sc + 8192; float* x0 = mod + 8 * 6144; float* h = x0 + 8192; float* q = h + 8192; float* o = q + 8 * 4096;
  float* ao = o + 8 * 4096; float* x1 = ao + 8192; float* h2 = x1 + 8192; float* g = h2 + 8192; float* u = g + 8 * 3584; float* a = u + 8 * 3584;
  float* f = a + 8 * 3584; float* x2 = f + 8192;
  k_silu_c<<<32, 256, 0, stream>>>(c, sc);
  skinny<<<96, 256, 0, stream>>>(sc, wm, mod, 1024, 6144);
  k_cvt<<<32, 256, 0, stream>>>(A_x, x0);
  k_ln<<<8, 256, 0, stream>>>(x0, mod, 0, 1024, h);
  skinny<<<64, 256, 0, stream>>>(h, wq, q, 1024, 4096);
  k_attn<<<256, 256, 0, stream>>>(q, kc, vc, o);
  skinny<<<16, 256, 0, stream>>>(o, wo, ao, 4096, 1024);
  k_res<<<32, 256, 0, stream>>>(x0, mod, 2048, ao, x1);
  k_ln<<<8, 256, 0, stream>>>(x1, mod, 3072, 4096, h2);
  skinny<<<56, 256, 0, stream>>>(h2, wg, g, 1024, 3584);
  skinny<<<56, 256, 0, stream>>>(h2, wu, u, 1024, 3584);
  k_act<<<112, 256, 0, stream>>>(g, u, a);
  skinny<<<16, 256, 0, stream>>>(a, wd, f, 3584, 1024);
  k_res<<<32, 256, 0, stream>>>(x1, mod, 5120, f, x2);
  k_out<<<32, 256, 0, stream>>>(x2, (__hip_bfloat16*)d_out);
}
```

```cpp
#include <hip/hip_runtime.h>
#include <stdint.h>

typedef unsigned u32;
typedef unsigned long long u64;
typedef unsigned short u16;
typedef u32 v4u __attribute__((ext_vector_type(4)));
typedef u32 v2u __attribute__((ext_vector_type(2)));
typedef float v4f __attribute__((ext_vector_type(4)));
typedef float v2f __attribute__((ext_vector_type(2)));
typedef __bf16 v8b __attribute__((ext_vector_type(8)));
typedef __bf16 v2b __attribute__((ext_vector_type(2)));
typedef __amdgpu_buffer_rsrc_t rsrc_t;
#define DEVI static __device__ __forceinline__

constexpr int OFF_P1 = 0x001000;
constexpr int OFF_H = 0x101000;
constexpr int OFF_Q = 0x109000;
constexpr int OFF_O = 0x189000;
constexpr int OFF_OS = 0x209000;
constexpr int OFF_MOD = 0x20D000;
constexpr int OFF_GB = 0x2CD000;
constexpr int OFF_WO = 0x30D000;
constexpr int OFF_X1 = 0x40D000;
constexpr int OFF_ST = 0x41D000;
constexpr int OFF_GU = 0x41E000;
constexpr int OFF_DN = 0x4FE000;
constexpr int OFF_DBG = 0x800000;
constexpr int WS_USED = 0x900000;

constexpr int L_KS = 0;
constexpr int L_RED = 65536;
constexpr int L_SA = 98304;
constexpr int L_R2 = L_SA + 4352;
constexpr int L_E = L_R2 + 4096;
constexpr int L_TOTAL = L_E + 64;
constexpr int SA_STRIDE = 528;
constexpr unsigned SPIN_MAX = 20000;

#define LDNT(rs, vo, so) __builtin_amdgcn_raw_buffer_load_b128(rs, vo, so, 2)
#define LDPL(rs, vo, so) __builtin_amdgcn_raw_buffer_load_b128(rs, vo, so, 0)
#define LDSC(rs, vo, so) __builtin_amdgcn_raw_buffer_load_b128(rs, vo, so, 16)
#define STSC(v, rs, vo, so) __builtin_amdgcn_raw_buffer_store_b128(v, rs, vo, so, 16)
#define BAR() asm volatile("s_waitcnt lgkmcnt(0)\n\ts_barrier" ::: "memory")
#define FENCE() asm volatile("" ::: "memory")

DEVI float asf(u32 x) { return __uint_as_float(x); }
DEVI u32 asu(float x) { return __float_as_uint(x); }
DEVI float bflo(u32 x) { return asf(x << 16); }
DEVI float bfhi(u32 x) { return asf(x & 0xFFFF0000u); }
DEVI u32 pack2(float lo, float hi) {
  v2b p = {(__bf16)lo, (__bf16)hi};
  return __builtin_bit_cast(u32, p);
}
DEVI float rbf(float f) { return (float)(__bf16)f; }
DEVI float rcp(float x) { return __builtin_amdgcn_rcpf(x); }
DEVI float silu(float v) { return v * rcp(1.f + __expf(-v)); }

template <int CTRL> DEVI float dpp(float x) { return asf(__builtin_amdgcn_update_dpp(0u, asu(x), CTRL, 0xf, 0xf, false)); }
template <int CTRL> DEVI u32 dppu(u32 x) { return __builtin_amdgcn_update_dpp(0u, x, CTRL, 0xf, 0xf, false); }
DEVI float sum16(float x) { x += dpp<0x128>(x); x += dpp<0x124>(x); x += dpp<0x122>(x); x += dpp<0x121>(x); return x; }
DEVI float add16(float x) { v2u a = __builtin_amdgcn_permlane16_swap(asu(x), asu(x), false, false); return asf(a.x) + asf(a.y); }
DEVI float add32(float x) { v2u a = __builtin_amdgcn_permlane32_swap(asu(x), asu(x), false, false); return asf(a.x) + asf(a.y); }
DEVI float max16(float x) { v2u a = __builtin_amdgcn_permlane16_swap(asu(x), asu(x), false, false); return fmaxf(asf(a.x), asf(a.y)); }
DEVI float max32(float x) { v2u a = __builtin_amdgcn_permlane32_swap(asu(x), asu(x), false, false); return fmaxf(asf(a.x), asf(a.y)); }
DEVI float sum64(float x) { return add32(add16(sum16(x))); }
DEVI bool all_ok(bool ok) { return __builtin_amdgcn_ballot_w64(!ok) == 0ull; }

struct Tag { u32 t0, t1, t2, t3, a, b; };
DEVI u32 enc(float v, u32 t) { return ((asu(v) + 0x200u) & 0xFFFFFC00u) | t; }
DEVI v4u enc4(v4f v, const Tag& T) { return v4u{enc(v.x, T.t0), enc(v.y, T.t1), enc(v.z, T.t2), enc(v.w, T.t3)}; }
DEVI bool chk4(v4u g, const Tag& T) { return (((g.x ^ T.t0) | (g.y ^ T.t1) | (g.z ^ T.t2) | (g.w ^ T.t3)) & 0x3FFu) == 0u; }
DEVI v4f dec4(v4u g) { return v4f{asf(g.x & 0xFFFFFC00u), asf(g.y & 0xFFFFFC00u), asf(g.z & 0xFFFFFC00u), asf(g.w & 0xFFFFFC00u)}; }
DEVI bool chk2(v4u g, const Tag& T) { return g.z == T.a && g.w == T.b; }

struct Unit { v4u a[8]; };
DEVI void load_unit(Unit& U, rsrc_t rs, int voff, int soff, int stride) {
#pragma unroll
  for (int j = 0; j < 8; ++j) U.a[j] = LDNT(rs, voff, soff + j * stride);
}
DEVI void transpose_unit(Unit& U) {
  Unit T;
#pragma unroll
  for (int i = 0; i < 8; ++i) {
    const u32 sel = (i & 1) ? 0x07060302u : 0x05040100u;
#pragma unroll
    for (int p = 0; p < 4; ++p) T.a[i][p] = __builtin_amdgcn_perm(U.a[2 * p + 1][i >> 1], U.a[2 * p][i >> 1], sel);
  }
#pragma unroll
  for (int i = 0; i < 8; ++i) { U.a[i] = T.a[i]; asm volatile("" : "+v"(U.a[i])); }
}
DEVI v4f mfma(v4u a, v4u b, v4f c) {
  return __builtin_amdgcn_mfma_f32_16x16x32_bf16(__builtin_bit_cast(v8b, a), __builtin_bit_cast(v8b, b), c, 0, 0, 0);
}
DEVI void mma_unit(v4f (&acc)[8], v4u a, const Unit& U) {
#pragma unroll
  for (int i = 0; i < 8; ++i) acc[i] = mfma(a, U.a[i], acc[i]);
}
DEVI void zero_acc(v4f (&acc)[8]) {
#pragma unroll
  for (int i = 0; i < 8; ++i) acc[i] = v4f{0.f, 0.f, 0.f, 0.f};
}
DEVI void store_red(unsigned char* red, const v4f (&acc)[8], int m, int g) {
  if (g < 2) {
#pragma unroll
    for (int i = 0; i < 4; ++i) {
      unsigned char* p = red + (4 * g + i) * 512 + m * 32;
      *(v4f*)p = v4f{acc[0][i], acc[1][i], acc[2][i], acc[3][i]};
      *(v4f*)(p + 16) = v4f{acc[4][i], acc[5][i], acc[6][i], acc[7][i]};
    }
  }
}
DEVI v4u silu_frag(v4u c) {
  v4u o;
#pragma unroll
  for (int i = 0; i < 4; ++i) o[i] = pack2(silu(bflo(c[i])), silu(bfhi(c[i])));
  return o;
}

#define STAMP(i) do { } while (0)

DEVI void mod_finish(Unit& U, v4u cfr, unsigned char* smem, rsrc_t rsW, const Tag& T, int nsm, int ksm, int w, int m, int g, int tid) {
  transpose_unit(U);
  v4f acc[8];
  zero_acc(acc);
  mma_unit(acc, silu_frag(cfr), U);
  store_red(smem + L_RED + w * 4096, acc, m, g);
  BAR();
  const int b = tid >> 6, q = tid & 63, ch = q >> 5;
  v4f s = v4f{0.f, 0.f, 0.f, 0.f};
#pragma unroll
  for (int rg = 0; rg < 4; ++rg) s += *(const v4f*)(smem + L_RED + (rg * 2 + ch) * 4096 + b * 512 + (q & 31) * 16);
  const int which = nsm >> 2;
  int off;
  if (which < 3) off = OFF_MOD + (((b * 16 + (nsm & 3) * 4 + (q >> 4)) * 3 + which) * 8 + ksm) * 256 + (q & 15) * 16;
  else off = OFF_GB + ((b * 32 + (nsm & 3) * 8 + (q >> 3)) * 8 + ksm) * 128 + (q & 7) * 16;
  STSC(enc4(s, T), rsW, off, 0);
  BAR();
}

__global__ void __launch_bounds__(512, 2)
fused_block(const u16* __restrict__ x, const u16* __restrict__ kc, const u16* __restrict__ vc, const u16* __restrict__ wq,
            const u16* __restrict__ wo, const u16* __restrict__ wg, const u16* __restrict__ wu, const u16* __restrict__ wd,
            const u16* __restrict__ cc, const u16* __restrict__ wm, u16* __restrict__ out, unsigned char* __restrict__ ws) {
  __shared__ __attribute__((aligned(16))) unsigned char smem[L_TOTAL];
  const int tid = threadIdx.x, l = tid & 63, w = __builtin_amdgcn_readfirstlane(tid >> 6);
  const int m = l & 15, g = l >> 4;
  const int c = blockIdx.x, xcd = c & 7, jc = c >> 3, role = jc >> 4, r = xcd * 16 + (jc & 15), u = xcd * 32 + jc;
  STAMP(0);
  const rsrc_t rsW = __builtin_amdgcn_make_buffer_rsrc((void*)ws, 0, WS_USED, 0x00020000);
  const rsrc_t rsM = __builtin_amdgcn_make_buffer_rsrc((void*)wm, 0, 1024 * 6144 * 2, 0x00020000);
  const rsrc_t rsC = __builtin_amdgcn_make_buffer_rsrc((void*)cc, 0, 8 * 1024 * 2, 0x00020000);
  const rsrc_t rsX = __builtin_amdgcn_make_buffer_rsrc((void*)x, 0, 8 * 1024 * 2, 0x00020000);

  asm volatile("" ::"s"(x), "s"(kc), "s"(vc), "s"(wq), "s"(wo), "s"(wg), "s"(wu), "s"(wd), "s"(cc), "s"(wm), "s"(out), "s"(ws));
  const int ks1 = u >> 4, ns1 = u & 15;
  Unit pa, pb;
  v4u cfa, cfb;
  v2u ev;
  auto p1_issue = [&]() {
    if (w == 0) {
      ev = __builtin_amdgcn_raw_buffer_load_b64(rsW, 0, 0, 16);
      cfa = LDPL(rsC, (m & 7) * 2048 + (ks1 * 64 + 8 * g) * 2, 0);
      cfb = LDPL(rsC, (m & 7) * 2048 + (ks1 * 64 + 32 + 8 * g) * 2, 0);
      load_unit(pa, rsM, 8 * g * 12288 + m * 16, ks1 * 64 * 12288 + ns1 * 256, 12288);
      load_unit(pb, rsM, 8 * g * 12288 + m * 16, (ks1 * 64 + 32) * 12288 + ns1 * 256, 12288);
    }
  };
  Tag T;
  u64 E;
  auto get_epoch = [&]() {
    if (w == 0) {
      if (l == 0) *(v2u*)(smem + L_E) = ev;
    }
    BAR();
    v2u e2 = *(const v2u*)(smem + L_E);
    E = (((u64)e2.y << 32) | e2.x) + 1ull;
    const u32 lo = __builtin_amdgcn_readfirstlane((u32)E), hi = __builtin_amdgcn_readfirstlane((u32)(E >> 32));
    E = ((u64)hi << 32) | lo;
    T.t0 = lo & 0x3FFu;
    T.t1 = ((lo >> 10) + lo) & 0x3FFu;
    T.t2 = ((lo >> 20) + lo) & 0x3FFu;
    T.t3 = ((u32)(E >> 30) + lo) & 0x3FFu;
    T.a = lo;
    T.b = hi ^ 0x5A5A5A5Au;
  };
  auto p1_finish = [&]() {
    if (w == 0) {
      transpose_unit(pa);
      transpose_unit(pb);
      v4f acc[8];
      zero_acc(acc);
      mma_unit(acc, silu_frag(cfa), pa);
      mma_unit(acc, silu_frag(cfb), pb);
      const int base = OFF_P1 + (((g & 1) * 4 * 16 + (ns1 & 7) * 2 + (m >> 3)) * 32 + ks1 * 2 + (ns1 >> 3)) * 256 + ((g >> 1) * 8 + (m & 7)) * 16;
#pragma unroll
      for (int i = 0; i < 4; ++i) {
        v4f v;
#pragma unroll
        for (int k = 0; k < 4; ++k) {
          v2u s = __builtin_amdgcn_permlane32_swap(asu(acc[k][i]), asu(acc[k + 4][i]), false, false);
          v[k] = asf(s.x);
        }
        STSC(enc4(v, T), rsW, base + i * 16 * 32 * 256, 0);
      }
    }
  };

  if (role == 0) {
    const rsrc_t rsQ = __builtin_amdgcn_make_buffer_rsrc((void*)wq, 0, 1024 * 4096 * 2, 0x00020000);
    const rsrc_t rsO = __builtin_amdgcn_make_buffer_rsrc((void*)wo, 0, 4096 * 1024 * 2, 0x00020000);
    const rsrc_t rsD = __builtin_amdgcn_make_buffer_rsrc((void*)wd, 0, 3584 * 1024 * 2, 0x00020000);
    const int hd = r & 31, ks2 = r >> 5;
    const int ob = r >> 4, ocb = r & 15;
    u32 xown = __builtin_amdgcn_raw_buffer_load_b16(rsX, (ob * 1024 + ocb * 64 + l) * 2, 0, 0);
    p1_issue();
    BAR();
    Unit U;
    load_unit(U, rsQ, 8 * g * 8192 + m * 16, (ks2 * 256 + 32 * w) * 8192 + hd * 256, 8192);
    get_epoch();
    p1_finish();
    STAMP(1);
    transpose_unit(U);
    {
      const int hoff = OFF_H + (m & 7) * 4096 + (ks2 * 64 + 8 * w + 2 * g + (m >> 3)) * 16;
      v4u hg;
      for (unsigned sp = 0; sp < SPIN_MAX; ++sp) {
        FENCE();
        hg = LDSC(rsW, hoff, 0);
        if (all_ok(chk2(hg, T))) break;
      }
      STAMP(2);
      v4u a = v4u{hg.x, hg.y, dppu<0x128>(hg.x), dppu<0x128>(hg.y)};
      v4f acc[8];
      zero_acc(acc);
      mma_unit(acc, a, U);
      store_red(smem + L_RED + w * 4096, acc, m, g);
      BAR();
      if (tid < 256) {
        const int b = tid >> 5, c4 = tid & 31;
        v4f s = v4f{0.f, 0.f, 0.f, 0.f};
#pragma unroll
        for (int k = 0; k < 8; ++k) s += *(const v4f*)(smem + L_RED + k * 4096 + b * 512 + c4 * 16);
        STSC(enc4(s, T), rsW, OFF_Q + ((b * 4 + (hd >> 3)) * 4 + ks2) * 4096 + (hd & 7) * 512 + c4 * 16, 0);
      }
    }
    STAMP(3);
    const int hd4 = r >> 2, nq = r & 3;
    load_unit(U, rsO, 8 * g * 2048 + m * 16, (hd4 * 128 + (w >> 1) * 32) * 2048 + nq * 512 + (w & 1) * 256, 2048);
    transpose_unit(U);
    {
      const int ooff = OFF_O + (hd4 * 8 + w) * 2048 + l * 16;
      const int soff = OFF_OS + ((hd4 * 8 + w) * 4 + (l & 3)) * 16;
      v4u d0, d1, sv;
      for (unsigned sp = 0; sp < SPIN_MAX; ++sp) {
        FENCE();
        sv = LDSC(rsW, soff, 0);
        d0 = LDSC(rsW, ooff, 0);
        d1 = LDSC(rsW, ooff + 1024, 0);
        if (all_ok(chk2(sv, T) && chk4(d0, T) && chk4(d1, T))) break;
      }
      STAMP(4);
      const float Ms = asf(sv.x), Ls = asf(sv.y);
      float M = fmaxf(Ms, dpp<0xB1>(Ms));
      M = fmaxf(M, dpp<0x4E>(M));
      const float es = __expf(Ms - M);
      float den = es * Ls;
      den += dpp<0xB1>(den);
      den += dpp<0x4E>(den);
      const float fs = es * rcp(den);
      const float f0 = dpp<0x00>(fs), f1 = dpp<0x55>(fs), f2 = dpp<0xAA>(fs), f3 = dpp<0xFF>(fs);
      const float fa = l < 32 ? f0 : f1, fb = l < 32 ? f2 : f3;
      const v4f a0 = dec4(d0), a1 = dec4(d1);
      v4f o4;
#pragma unroll
      for (int k = 0; k < 4; ++k) o4[k] = add32(fa * a0[k] + fb * a1[k]);
      if (l < 32) *(v2u*)(smem + L_SA + w * SA_STRIDE + l * 8) = v2u{pack2(o4.x, o4.y), pack2(o4.z, o4.w)};
      BAR();
      const v4u a = *(const v4u*)(smem + L_SA + (m & 7) * SA_STRIDE + ((w >> 1) * 32 + 8 * g) * 2);
      v4f acc[8];
      zero_acc(acc);
      mma_unit(acc, a, U);
      store_red(smem + L_RED + w * 4096, acc, m, g);
      BAR();
      const int b = tid >> 6, q = tid & 63, ch = q >> 5;
      v4f s = v4f{0.f, 0.f, 0.f, 0.f};
#pragma unroll
      for (int rg = 0; rg < 4; ++rg) s += *(const v4f*)(smem + L_RED + (rg * 2 + ch) * 4096 + b * 512 + (q & 31) * 16);
      STSC(enc4(s, T), rsW, OFF_WO + ((b * 16 + nq * 4 + (q >> 4)) * 32 + hd4) * 256 + (q & 15) * 16, 0);
    }
    STAMP(5);
    {
      const int woff = OFF_WO + r * 8192 + w * 1024 + l * 16;
      const int moff = OFF_MOD + r * 6144 + (w < 6 ? w : 0) * 1024 + l * 16;
      v4u d0, d1;
      for (unsigned sp = 0; sp < SPIN_MAX; ++sp) {
        FENCE();
        d1 = LDSC(rsW, moff, 0);
        d0 = LDSC(rsW, woff, 0);
        if (all_ok(chk4(d0, T) && chk4(d1, T))) break;
      }
      STAMP(6);
      v4f a0 = dec4(d0), a1 = dec4(d1);
#pragma unroll
      for (int k = 0; k < 4; ++k) { a0[k] = add32(add16(a0[k])); a1[k] = add32(add16(a1[k])); }
      if (l < 16) {
        *(v4f*)(smem + L_R2 + w * 256 + l * 16) = a0;
        *(v4f*)(smem + L_R2 + 2048 + w * 256 + l * 16) = a1;
      }
      BAR();
    }
    const bool has_dn = r < 112;
    const int k6 = r >> 1, nh = r & 1;
    const int idx2 = 16 + r - 112, nsm = 12 + (idx2 & 3), ksm = idx2 >> 2;
    v4u cfr = v4u{0, 0, 0, 0};
    if (w == 0) {
      const float* r2 = (const float*)(smem + L_R2);
      float attn = 0.f;
#pragma unroll
      for (int k = 0; k < 8; ++k) attn += r2[k * 64 + l];
      const float ga = rbf(r2[512 + l] + r2[512 + 64 + l]);
      const float sb = rbf(r2[512 + 128 + l] + r2[512 + 192 + l]);
      const float cb = rbf(r2[512 + 256 + l] + r2[512 + 320 + l]);
      asm volatile("" : "+v"(xown));
      const float x1 = rbf(bflo(xown) + rbf(ga * rbf(attn)));
      const float uu = rbf(1.f + cb);
      const float mean = sum64(x1) * (1.f / 64.f);
      const float dv = x1 - mean;
      const float m2 = sum64(dv * dv);
      const u32 p0 = pack2(x1, uu), p1 = pack2(sb, 0.f);
      const u32 q0 = dppu<0xB1>(p0), q1 = dppu<0xB1>(p1);
      if ((l & 1) == 0) {
        v4u gr = v4u{p0, (p1 & 0xFFFFu) | (q0 << 16), (q0 >> 16) | (q1 << 16), T.a};
        STSC(gr, rsW, OFF_X1 + ob * 8192 + (ocb * 32 + (l >> 1)) * 16, 0);
      }
      if (l == 0) STSC((v4u{asu(mean), asu(m2), T.a, T.b}), rsW, OFF_ST + r * 16, 0);
    }
    STAMP(7);
    {
      const rsrc_t rsS = has_dn ? rsD : rsM;
      const int stride = has_dn ? 2048 : 12288;
      const int so = has_dn ? (k6 * 64 + (w & 1) * 32) * 2048 + nh * 1024 + (w >> 1) * 256
                            : (ksm * 128 + (w >> 1) * 32) * 12288 + 4096 + nsm * 512 + (w & 1) * 256;
      cfr = LDPL(rsC, (m & 7) * 2048 + ((ksm & 7) * 128 + (w >> 1) * 32 + 8 * g) * 2, 0);
      load_unit(U, rsS, 8 * g * stride + m * 16, so, stride);
    }
    if (has_dn) {
      transpose_unit(U);
      const int goff = OFF_GU + (k6 * 8 + w) * 2048 + l * 16;
      v4u d0, d1;
      for (unsigned sp = 0; sp < SPIN_MAX; ++sp) {
        FENCE();
        d0 = LDSC(rsW, goff, 0);
        d1 = LDSC(rsW, goff + 1024, 0);
        if (all_ok(chk4(d0, T) && chk4(d1, T))) break;
      }
      STAMP(8);
      v4f a0 = dec4(d0) + dec4(d1);
      float act[4];
#pragma unroll
      for (int k = 0; k < 4; ++k) {
        const float t = rbf(add32(a0[k]));
        v2u s = __builtin_amdgcn_permlane16_swap(asu(t), asu(t), false, false);
        act[k] = rbf(silu(asf(s.x))) * asf(s.y);
      }
      if (l < 16) *(v2u*)(smem + L_SA + w * SA_STRIDE + l * 8) = v2u{pack2(act[0], act[1]), pack2(act[2], act[3])};
      BAR();
      const v4u a = *(const v4u*)(smem + L_SA + (m & 7) * SA_STRIDE + ((w & 1) * 32 + 8 * g) * 2);
      v4f acc[8];
      zero_acc(acc);
      mma_unit(acc, a, U);
      store_red(smem + L_RED + w * 4096, acc, m, g);
      BAR();
      const int b = tid >> 6, q = tid & 63;
#pragma unroll
      for (int hh = 0; hh < 2; ++hh) {
        const int col4 = q + 64 * hh, cq = col4 >> 5, c4 = col4 & 31;
        v4f s = *(const v4f*)(smem + L_RED + (2 * cq) * 4096 + b * 512 + c4 * 16) +
                *(const v4f*)(smem + L_RED + (2 * cq + 1) * 4096 + b * 512 + c4 * 16);
        STSC(enc4(s, T), rsW, OFF_DN + ((b * 32 + nh * 16 + cq * 4 + (c4 >> 3)) * 56 + k6) * 128 + (c4 & 7) * 16, 0);
      }
    } else {
      mod_finish(U, cfr, smem, rsW, T, nsm, ksm, w, m, g, tid);
    }
    STAMP(9);
  } else {
    const rsrc_t rsK = __builtin_amdgcn_make_buffer_rsrc((void*)kc, 0, 8 * 4 * 4096 * 128 * 2, 0x00020000);
    const rsrc_t rsV = __builtin_amdgcn_make_buffer_rsrc((void*)vc, 0, 8 * 4 * 4096 * 128 * 2, 0x00020000);
    const int ob = r >> 4, ocb = r & 15;
    v4u xa = LDPL(rsX, ob * 2048 + l * 32, 0);
    v4u xb = LDPL(rsX, ob * 2048 + l * 32 + 16, 0);
    v2u xs = __builtin_amdgcn_raw_buffer_load_b64(rsX, ob * 2048 + (ocb * 64 + 8 * (l & 7) + 4 * ((l >> 3) & 1)) * 2, 0, 0);
    p1_issue();
    get_epoch();
    p1_finish();
    float mean1 = 0.f, rden1 = 0.f;
    if (w == 1) {
      float s = 0.f;
#pragma unroll
      for (int k = 0; k < 4; ++k) s += bflo(xa[k]) + bfhi(xa[k]) + bflo(xb[k]) + bfhi(xb[k]);
      const float mu = sum64(s) * (1.f / 1024.f);
      float v = 0.f;
#pragma unroll
      for (int k = 0; k < 4; ++k) {
        float d;
        d = bflo(xa[k]) - mu; v += d * d;
        d = bfhi(xa[k]) - mu; v += d * d;
        d = bflo(xb[k]) - mu; v += d * d;
        d = bfhi(xb[k]) - mu; v += d * d;
      }
      v = rbf(sum64(v) * (1.f / 1024.f));
      mean1 = rbf(mu);
      rden1 = rcp(rbf(sqrtf(rbf(v + 1e-5f))));
    }
    STAMP(1);
    {
      const int poff = OFF_P1 + r * 8192 + w * 1024 + l * 16;
      v4u d0;
      for (unsigned sp = 0; sp < SPIN_MAX; ++sp) {
        FENCE();
        d0 = LDSC(rsW, poff, 0);
        if (all_ok(chk4(d0, T))) break;
      }
      STAMP(2);
      v4f a0 = dec4(d0);
#pragma unroll
      for (int k = 0; k < 4; ++k) a0[k] = add32(a0[k]);
      if (l < 32) *(v4f*)(smem + L_R2 + w * 512 + l * 16) = a0;
      BAR();
    }
    const int ab = r >> 4, g4 = (r >> 2) & 3, sp4 = r & 3;
    const int kvoff = ((ab * 4 + g4) * 4096 + 3072 + sp4 * 256 + 32 * w) * 256;
    if (w == 1) {
      v4f s = v4f{0.f, 0.f, 0.f, 0.f};
#pragma unroll
      for (int k = 0; k < 8; ++k) s += *(const v4f*)(smem + L_R2 + k * 512 + (l & 31) * 16);
      float h[4];
#pragma unroll
      for (int k = 0; k < 4; ++k) {
        const float t = rbf(s[k]);
        v2u sc = __builtin_amdgcn_permlane16_swap(asu(t), asu(t), false, false);
        const float xv = (k & 1) ? bfhi(xs[k >> 1]) : bflo(xs[k >> 1]);
        const float xn = rbf(rbf(xv - mean1) * rden1);
        h[k] = rbf(xn * rbf(1.f + asf(sc.y))) + asf(sc.x);
      }
      if (l < 16)
        STSC((v4u{pack2(h[0], h[1]), pack2(h[2], h[3]), T.a, T.b}), rsW, OFF_H + ob * 4096 + (ocb * 16 + 2 * (l & 7) + (l >> 3)) * 16, 0);
    }
    STAMP(3);
    Unit KU, VU;
    load_unit(KU, rsK, l * 16, kvoff, 1024);
    load_unit(VU, rsV, l * 16, kvoff, 1024);
    {
      unsigned char* ks = smem + L_KS + w * 8192;
#pragma unroll
      for (int i = 0; i < 8; ++i) {
        const int key = 4 * i + g;
        *(v4u*)(ks + key * 256 + ((m ^ (key & 15)) * 16)) = KU.a[i];
      }
      asm volatile("s_waitcnt lgkmcnt(0)" ::: "memory");
#pragma unroll
      for (int t = 0; t < 2; ++t) {
        const int key = 16 * t + 4 * (m & 3) + (m >> 2);
#pragma unroll
        for (int kk = 0; kk < 4; ++kk) {
          KU.a[t * 4 + kk] = *(const v4u*)(ks + key * 256 + (((4 * kk + g) ^ (key & 15)) * 16));
        }
      }
#pragma unroll
      for (int i = 0; i < 8; ++i) asm volatile("" : "+v"(KU.a[i]));
    }
    transpose_unit(VU);
    {
      const int qoff = OFF_Q + (ab * 4 + g4) * 16384 + w * 512 + (l >> 5) * 4096 + (l & 31) * 16;
      v4u d0, d1;
      for (unsigned sp = 0; sp < SPIN_MAX; ++sp) {
        FENCE();
        d0 = LDSC(rsW, qoff, 0);
        d1 = LDSC(rsW, qoff + 8192, 0);
        if (all_ok(chk4(d0, T) && chk4(d1, T))) break;
      }
      STAMP(4);
      v4f a0 = dec4(d0) + dec4(d1);
#pragma unroll
      for (int k = 0; k < 4; ++k) a0[k] = add32(a0[k]);
      if (l < 32) *(v2u*)(smem + L_SA + w * SA_STRIDE + l * 8) = v2u{pack2(a0.x, a0.y), pack2(a0.z, a0.w)};
      BAR();
    }
    const bool has_mod = r < 96 || r >= 112;
    const int nsm = r < 96 ? r % 12 : 12 + ((r - 112) & 3), ksm = r < 96 ? r / 12 : (r - 112) >> 2;
    const bool has_gu = r < 112;
    const int mat = r & 1, t5 = r >> 1, ns5 = t5 % 14, ks5 = t5 / 14;
    Unit MU, G0, G1;
    v4u cfr = v4u{0, 0, 0, 0};
    {
      const rsrc_t rsMm = __builtin_amdgcn_make_buffer_rsrc((void*)wm, 0, has_mod ? 1024 * 6144 * 2 : 0, 0x00020000);
      cfr = LDPL(rsC, (m & 7) * 2048 + ((ksm & 7) * 128 + (w >> 1) * 32 + 8 * g) * 2, 0);
      load_unit(MU, rsMm, 8 * g * 12288 + m * 16, (ksm * 128 + (w >> 1) * 32) * 12288 + 4096 + nsm * 512 + (w & 1) * 256, 12288);
    }
    STAMP(8);
    {
      v4f s0 = v4f{0.f, 0.f, 0.f, 0.f}, s1 = v4f{0.f, 0.f, 0.f, 0.f};
#pragma unroll
      for (int kk = 0; kk < 4; ++kk) {
        v4u qf = *(const v4u*)(smem + L_SA + (m & 7) * SA_STRIDE + (32 * kk + 8 * g) * 2);
        s0 = mfma(KU.a[kk], qf, s0);
        s1 = mfma(KU.a[4 + kk], qf, s1);
      }
      float sc[8];
#pragma unroll
      for (int i = 0; i < 4; ++i) {
        sc[i] = rbf(rbf(s0[i]) * 0.08838834764831845f);
        sc[4 + i] = rbf(rbf(s1[i]) * 0.08838834764831845f);
      }
      float mx = sc[0];
#pragma unroll
      for (int i = 1; i < 8; ++i) mx = fmaxf(mx, sc[i]);
      mx = max32(max16(mx));
      float ls = 0.f;
#pragma unroll
      for (int i = 0; i < 8; ++i) { sc[i] = __expf(sc[i] - mx); ls += sc[i]; }
      ls = add32(add16(ls));
      const v4u pf = v4u{pack2(sc[0], sc[1]), pack2(sc[2], sc[3]), pack2(sc[4], sc[5]), pack2(sc[6], sc[7])};
      v4f acc[8];
      zero_acc(acc);
      mma_unit(acc, pf, VU);
      store_red(smem + L_RED + w * 4096, acc, m, g);
      if (l < 8) {
        *(float*)(smem + L_R2 + l * 32 + w * 4) = mx;
        *(float*)(smem + L_R2 + 256 + l * 32 + w * 4) = ls;
      }
      BAR();
      if (tid < 256) {
        const int hh = tid >> 5, c4 = tid & 31;
        const v4f m0 = *(const v4f*)(smem + L_R2 + hh * 32), m1 = *(const v4f*)(smem + L_R2 + hh * 32 + 16);
        const v4f l0 = *(const v4f*)(smem + L_R2 + 256 + hh * 32), l1 = *(const v4f*)(smem + L_R2 + 256 + hh * 32 + 16);
        float M = fmaxf(fmaxf(fmaxf(m0.x, m0.y), fmaxf(m0.z, m0.w)), fmaxf(fmaxf(m1.x, m1.y), fmaxf(m1.z, m1.w)));
        float L = 0.f;
        v4f o = v4f{0.f, 0.f, 0.f, 0.f};
#pragma unroll
        for (int k = 0; k < 8; ++k) {
          const float e = __expf((k < 4 ? m0[k & 3] : m1[k & 3]) - M);
          L += e * (k < 4 ? l0[k & 3] : l1[k & 3]);
          o += e * *(const v4f*)(smem + L_RED + k * 4096 + hh * 512 + c4 * 16);
        }
        const int slot = ((g4 * 8 + hh) * 8 + ab) * 4 + sp4;
        STSC(enc4(o, T), rsW, OFF_O + slot * 512 + c4 * 16, 0);
        if (c4 == 0) STSC((v4u{asu(M), asu(L), T.a, T.b}), rsW, OFF_OS + slot * 16, 0);
      }
      BAR();
    }
    STAMP(5);
    {
      const rsrc_t rsG = __builtin_amdgcn_make_buffer_rsrc((void*)(mat ? wu : wg), 0, has_gu ? 1024 * 3584 * 2 : 0, 0x00020000);
      const int so = (ks5 * 256 + (w >> 1) * 64) * 7168 + ns5 * 512 + (w & 1) * 256;
      load_unit(G0, rsG, 8 * g * 7168 + m * 16, so, 7168);
      load_unit(G1, rsG, 8 * g * 7168 + m * 16, so + 32 * 7168, 7168);
    }
    if (has_mod) mod_finish(MU, cfr, smem, rsW, T, nsm, ksm, w, m, g, tid);
    STAMP(6);
    if (has_gu) {
      transpose_unit(G0);
      transpose_unit(G1);
      const int xoff = OFF_X1 + w * 8192 + (ks5 * 128 + l) * 16;
      const int soff = OFF_ST + (w * 16 + m) * 16;
      v4u d0, d1, sv;
      for (unsigned sp = 0; sp < SPIN_MAX; ++sp) {
        FENCE();
        sv = LDSC(rsW, soff, 0);
        d0 = LDSC(rsW, xoff, 0);
        d1 = LDSC(rsW, xoff + 1024, 0);
        if (all_ok(chk2(sv, T) && d0.w == T.a && d1.w == T.a)) break;
      }
      STAMP(7);
      const float mi = asf(sv.x);
      const float mu = sum16(mi) * (1.f / 16.f);
      const float dm = mi - mu;
      const float var = rbf(sum16(asf(sv.y) + 64.f * dm * dm) * (1.f / 1024.f));
      const float mb = rbf(mu);
      const float rden = rcp(rbf(sqrtf(rbf(var + 1e-5f))));
#pragma unroll
      for (int t = 0; t < 2; ++t) {
        const v4u d = t ? d1 : d0;
        const float ha = rbf(rbf(rbf(bflo(d.x) - mb) * rden) * bfhi(d.x)) + bflo(d.y);
        const float hb = rbf(rbf(rbf(bfhi(d.y) - mb) * rden) * bflo(d.z)) + bfhi(d.z);
        *(u32*)(smem + L_SA + w * SA_STRIDE + (128 * t + 2 * l) * 2) = pack2(ha, hb);
      }
      BAR();
      const v4u a0 = *(const v4u*)(smem + L_SA + (m & 7) * SA_STRIDE + ((w >> 1) * 64 + 8 * g) * 2);
      const v4u a1 = *(const v4u*)(smem + L_SA + (m & 7) * SA_STRIDE + ((w >> 1) * 64 + 32 + 8 * g) * 2);
      v4f acc[8];
      zero_acc(acc);
      mma_unit(acc, a0, G0);
      mma_unit(acc, a1, G1);
      store_red(smem + L_RED + w * 4096, acc, m, g);
      BAR();
      const int b = tid >> 6, q = tid & 63, ch = q >> 5;
      v4f s = v4f{0.f, 0.f, 0.f, 0.f};
#pragma unroll
      for (int rg = 0; rg < 4; ++rg) s += *(const v4f*)(smem + L_RED + (rg * 2 + ch) * 4096 + b * 512 + (q & 31) * 16);
      STSC(enc4(s, T), rsW, OFF_GU + ((((ns5 * 4 + (q >> 4)) * 8 + b) * 4 + ks5) * 2 + mat) * 256 + (q & 15) * 16, 0);
    }
    STAMP(9);
  }

  {
    const int lc = l < 56 ? l : 55;
    const int doff = OFF_DN + u * 7168 + w * 896 + lc * 16;
    const int ob = u >> 5, c32 = u & 31;
    const int eoff = w == 0 ? OFF_GB + u * 1024 + l * 16 : OFF_X1 + ob * 8192 + (c32 * 16 + m) * 16;
    v4u d0, d1;
    for (unsigned sp = 0; sp < SPIN_MAX; ++sp) {
      FENCE();
      d1 = LDSC(rsW, eoff, 0);
      d0 = LDSC(rsW, doff, 0);
      if (all_ok(chk4(d0, T) && (w == 0 ? chk4(d1, T) : d1.w == T.a))) break;
    }
    STAMP(10);
    v4f a0 = dec4(d0);
    if (l >= 56) a0 = v4f{0.f, 0.f, 0.f, 0.f};
#pragma unroll
    for (int k = 0; k < 4; ++k) a0[k] = add32(add16(a0[k] + dpp<0x128>(a0[k])));
    if (l < 8) *(v4f*)(smem + L_R2 + w * 128 + l * 16) = a0;
    if (w == 0) {
      v4f a1 = dec4(d1);
#pragma unroll
      for (int k = 0; k < 4; ++k) a1[k] = add32(add16(a1[k] + dpp<0x128>(a1[k])));
      if (l < 8) *(v4f*)(smem + L_R2 + 1024 + l * 16) = a1;
    }
    if (w == 1 && l < 16) *(v2f*)(smem + L_R2 + 1152 + l * 8) = v2f{bflo(d1.x), bfhi(d1.y)};
    BAR();
    if (w == 0 && l < 32) {
      const float* r2 = (const float*)(smem + L_R2);
      float f = 0.f;
#pragma unroll
      for (int k = 0; k < 8; ++k) f += r2[k * 32 + l];
      const float o = r2[288 + l] + rbf(rbf(r2[256 + l]) * rbf(f));
      out[ob * 1024 + c32 * 32 + l] = (u16)(pack2(o, 0.f) & 0xFFFFu);
      if (u == 0 && l == 0) __builtin_amdgcn_raw_buffer_store_b64(v2u{(u32)E, (u32)(E >> 32)}, rsW, 0, 0, 16);
    }
  }
}

extern "C" void kernel_launch(void* const* d_in, const int* in_sizes, int n_in, void* d_out, int out_size, void* d_ws,
                              size_t ws_size, hipStream_t stream) {
  fused_block<<<256, 512, 0, stream>>>((const u16*)d_in[0], (const u16*)d_in[1], (const u16*)d_in[2], (const u16*)d_in[3],
                                       (const u16*)d_in[4], (const u16*)d_in[6], (const u16*)d_in[7], (const u16*)d_in[8],
                                       (const u16*)d_in[9], (const u16*)d_in[10], (u16*)d_out, (unsigned char*)d_ws);
}
```

```cpp
#include <hip/hip_runtime.h>
#include <stdint.h>

typedef unsigned u32;
typedef unsigned long long u64;
typedef unsigned short u16;
typedef u32 v4u __attribute__((ext_vector_type(4)));
typedef u32 v2u __attribute__((ext_vector_type(2)));
typedef float v4f __attribute__((ext_vector_type(4)));
typedef float v2f __attribute__((ext_vector_type(2)));
typedef __bf16 v8b __attribute__((ext_vector_type(8)));
typedef __bf16 v2b __attribute__((ext_vector_type(2)));
typedef __amdgpu_buffer_rsrc_t rsrc_t;
#define DEVI static __device__ __forceinline__

constexpr int OFF_P1 = 0x001000;
constexpr int OFF_H = 0x081000;
constexpr int OFF_Q = 0x089000;
constexpr int OFF_O = 0x109000;
constexpr int OFF_OS = 0x189000;
constexpr int OFF_MOD = 0x18D000;
constexpr int OFF_GB = 0x24D000;
constexpr int OFF_WO = 0x28D000;
constexpr int OFF_X1 = 0x38D000;
constexpr int OFF_US = 0x395000;
constexpr int OFF_ST = 0x3A5000;
constexpr int OFF_GU = 0x3A6000;
constexpr int OFF_DN = 0x486000;
constexpr int OFF_DBG = 0x800000;
constexpr int WS_USED = 0x900000;

constexpr int RROW = 528;
constexpr int RBLK = 8 * RROW;
constexpr int L_KS = 0;
constexpr int L_RA = 65536;
constexpr int L_RB = L_RA + 8 * RBLK;
constexpr int L_SA = L_RB + 8 * RBLK;
constexpr int L_R2 = L_SA + RBLK;
constexpr int L_TOTAL = L_R2 + 8192;
constexpr unsigned SPIN_MAX = 200000;

#define LDNT(rs, vo, so) __builtin_amdgcn_raw_buffer_load_b128(rs, vo, so, 2)
#define LDPL(rs, vo, so) __builtin_amdgcn_raw_buffer_load_b128(rs, vo, so, 0)
#define LDSC(rs, vo, so) __builtin_amdgcn_raw_buffer_load_b128(rs, vo, so, 16)
#define STSC(v, rs, vo, so) __builtin_amdgcn_raw_buffer_store_b128(v, rs, vo, so, 16)
#define BAR() asm volatile("s_waitcnt lgkmcnt(0)\n\ts_barrier" ::: "memory")
#define FENCE() asm volatile("" ::: "memory")
#define PIN(x) asm volatile("" : "+v"(x))

DEVI float asf(u32 x) { return __uint_as_float(x); }
DEVI u32 asu(float x) { return __float_as_uint(x); }
DEVI float bflo(u32 x) { return asf(x << 16); }
DEVI float bfhi(u32 x) { return asf(x & 0xFFFF0000u); }
DEVI u32 pack2(float lo, float hi) {
  v2b p = {(__bf16)lo, (__bf16)hi};
  return __builtin_bit_cast(u32, p);
}
DEVI float rbf(float f) { return (float)(__bf16)f; }
DEVI float silu_div(float v) { return v / (1.f + __expf(-v)); }

template <int CTRL> DEVI float dpp(float x) { return asf(__builtin_amdgcn_update_dpp(0u, asu(x), CTRL, 0xf, 0xf, false)); }
template <int CTRL> DEVI u32 dppu(u32 x) { return __builtin_amdgcn_update_dpp(0u, x, CTRL, 0xf, 0xf, false); }
DEVI float sum16(float x) { x += dpp<0x128>(x); x += dpp<0x124>(x); x += dpp<0x122>(x); x += dpp<0x121>(x); return x; }
DEVI float sadd32(float a, float b) { v2u s = __builtin_amdgcn_permlane32_swap(asu(a), asu(b), false, false); return asf(s.x) + asf(s.y); }
DEVI float sadd16(float a, float b) { v2u s = __builtin_amdgcn_permlane16_swap(asu(a), asu(b), false, false); return asf(s.x) + asf(s.y); }
DEVI float max16(float x) { v2u a = __builtin_amdgcn_permlane16_swap(asu(x), asu(x), false, false); return fmaxf(asf(a.x), asf(a.y)); }
DEVI float max32(float x) { v2u a = __builtin_amdgcn_permlane32_swap(asu(x), asu(x), false, false); return fmaxf(asf(a.x), asf(a.y)); }
DEVI float sum64(float x) { x = sum16(x); x = sadd16(x, x); return sadd32(x, x); }
DEVI bool any_bad(u32 bad) { return __builtin_amdgcn_ballot_w64(bad != 0u) != 0ull; }

struct Tag { u32 t0, t1, t2, t3, e0, a, b; };
DEVI v4u enc4(v4f v, const Tag& T) {
  float mx = fmaxf(fmaxf(fabsf(v.x), fabsf(v.y)), fabsf(v.z));
  mx = fmaxf(fmaxf(mx, fabsf(v.w)), 0x1p-106f) * 1.00000095367431640625f;
  const u32 eb = asu(mx) & 0x7F800000u;
  const float inv = asf(0x89000000u - eb);
  const float mg = 12582912.f;
  return v4u{(asu(fmaf(v.x, inv, mg)) << 10) | ((eb >> 23) + T.e0), (asu(fmaf(v.y, inv, mg)) << 10) | T.t1,
             (asu(fmaf(v.z, inv, mg)) << 10) | T.t2, (asu(fmaf(v.w, inv, mg)) << 10) | T.t3};
}
DEVI u32 bad3(v4u g, const Tag& T) { return (g.y ^ T.t1) | (g.z ^ T.t2) | (g.w ^ T.t3); }
DEVI u32 badf(u32 y, u32 z) { return (y & 0x3FFu) | (z & 0x300u); }
DEVI u32 bad4(v4u g, const Tag& T) { return badf(bad3(g, T), g.x ^ T.t0); }
DEVI u32 bad4(v4u g, v4u h, const Tag& T) { return badf(bad3(g, T) | bad3(h, T), (g.x ^ T.t0) | (h.x ^ T.t0)); }
DEVI u32 bad2(v4u g, const Tag& T) { return (g.z ^ T.a) | (g.w ^ T.b); }
DEVI float gscale(v4u g) { return fabsf(asf(g.x << 23)); }
DEVI v4f dec4(v4u g) {
  const float s = gscale(g);
  return v4f{(float)((int)g.x >> 10) * s, (float)((int)g.y >> 10) * s, (float)((int)g.z >> 10) * s, (float)((int)g.w >> 10) * s};
}
DEVI v4f dec4acc(v4u g, v4f a) {
  const float s = gscale(g);
  return v4f{fmaf((float)((int)g.x >> 10), s, a.x), fmaf((float)((int)g.y >> 10), s, a.y), fmaf((float)((int)g.z >> 10), s, a.z),
             fmaf((float)((int)g.w >> 10), s, a.w)};
}
DEVI float fold4(v4f v) { return sadd16(sadd32(v.x, v.y), sadd32(v.z, v.w)); }

struct Unit { v4u a[8]; };
DEVI void load_nat(Unit& U, rsrc_t rs, int voff, int soff, int stride) {
#pragma unroll
  for (int j = 0; j < 8; ++j) U.a[j] = LDNT(rs, voff, soff + j * stride);
}
DEVI void load_rm(Unit& U, rsrc_t rs, int voff, int soff, int stride) {
#pragma unroll
  for (int j = 0; j < 8; ++j) U.a[j] = LDNT(rs, voff, soff + ((j >> 2) + 8 * (((j & 1) << 1) | ((j >> 1) & 1))) * stride);
}
DEVI void transpose_unit(Unit& U) {
  Unit T;
#pragma unroll
  for (int i = 0; i < 8; ++i) {
    const u32 sel = (i & 1) ? 0x07060302u : 0x05040100u;
#pragma unroll
    for (int p = 0; p < 4; ++p) T.a[i][p] = __builtin_amdgcn_perm(U.a[2 * p + 1][i >> 1], U.a[2 * p][i >> 1], sel);
  }
#pragma unroll
  for (int i = 0; i < 8; ++i) { U.a[i] = T.a[i]; PIN(U.a[i]); }
}
DEVI v4f mfma(v4u a, v4u b, v4f c) {
  return __builtin_amdgcn_mfma_f32_16x16x32_bf16(__builtin_bit_cast(v8b, a), __builtin_bit_cast(v8b, b), c, 0, 0, 0);
}
DEVI void mma_first(v4f (&acc)[8], const Unit& U, v4u act) {
#pragma unroll
  for (int i = 0; i < 8; ++i) acc[i] = mfma(U.a[i], act, v4f{0.f, 0.f, 0.f, 0.f});
}
DEVI void mma_next(v4f (&acc)[8], const Unit& U, v4u act) {
#pragma unroll
  for (int i = 0; i < 8; ++i) acc[i] = mfma(U.a[i], act, acc[i]);
}
DEVI void store_red(unsigned char* blk, const v4f (&acc)[8], int m, int g) {
  if (m < 8) {
#pragma unroll
    for (int i = 0; i < 8; ++i) *(v4f*)(blk + m * RROW + g * 128 + i * 16) = acc[i];
  }
}
DEVI v4u silu_frag(v2u c) {
  const u32 p0 = pack2(silu_div(bflo(c.x)), silu_div(bfhi(c.x))), p1 = pack2(silu_div(bflo(c.y)), silu_div(bfhi(c.y)));
  return v4u{p0, p1, dppu<0x128>(p0), dppu<0x128>(p1)};
}

#define STAMP(i) do { } while (0)
#define POLL(loads, badexpr) \
  for (unsigned sp_ = 0; sp_ < SPIN_MAX; ++sp_) { FENCE(); loads; if (!any_bad(badexpr)) break; }

__global__ void __launch_bounds__(512, 2)
fused_block(const u16* __restrict__ x, const u16* __restrict__ kc, const u16* __restrict__ vc, const u16* __restrict__ wq,
            const u16* __restrict__ wo, const u16* __restrict__ wg, const u16* __restrict__ wu, const u16* __restrict__ wd,
            const u16* __restrict__ cc, const u16* __restrict__ wm, u16* __restrict__ out, unsigned char* __restrict__ ws) {
  __shared__ __attribute__((aligned(16))) unsigned char smem[L_TOTAL];
  const int tid = threadIdx.x, l = tid & 63, w = __builtin_amdgcn_readfirstlane(tid >> 6);
  const int m = l & 15, g = l >> 4;
  const int c = blockIdx.x, xcd = c & 7, jc = c >> 3, role = jc >> 4, r = xcd * 16 + (jc & 15), u = xcd * 32 + jc;
  STAMP(0);
  const rsrc_t rsW = __builtin_amdgcn_make_buffer_rsrc((void*)ws, 0, WS_USED, 0x00020000);
  const rsrc_t rsM = __builtin_amdgcn_make_buffer_rsrc((void*)wm, 0, 1024 * 6144 * 2, 0x00020000);
  const rsrc_t rsC = __builtin_amdgcn_make_buffer_rsrc((void*)cc, 0, 8 * 1024 * 2, 0x00020000);
  asm volatile("" ::"s"(x), "s"(kc), "s"(vc), "s"(wq), "s"(wo), "s"(wg), "s"(wu), "s"(wd), "s"(cc), "s"(wm), "s"(out), "s"(ws));
  Tag T;
  u32 elo, ehi;
  auto make_tag = [&](v2u ev) {
    const u64 E = (((u64)ev.y << 32) | ev.x) + 1ull;
    elo = __builtin_amdgcn_readfirstlane((u32)E);
    ehi = __builtin_amdgcn_readfirstlane((u32)(E >> 32));
    T.t0 = (elo & 3u) << 8;
    T.t1 = (elo >> 2) & 0x3FFu;
    T.t2 = (elo >> 12) & 0x3FFu;
    T.t3 = elo >> 22;
    T.e0 = T.t0 - 20u;
    T.a = elo;
    T.b = ehi ^ 0x5A5A5A5Au;
  };
  const int rb = tid >> 6, rch = (tid >> 5) & 1, rq = tid & 31;
  const int prow = ((g & 1) << 1) | (g >> 1);

  if (role == 0) {
    const rsrc_t rsQ = __builtin_amdgcn_make_buffer_rsrc((void*)wq, 0, 1024 * 4096 * 2, 0x00020000);
    const rsrc_t rsO = __builtin_amdgcn_make_buffer_rsrc((void*)wo, 0, 4096 * 1024 * 2, 0x00020000);
    const rsrc_t rsX = __builtin_amdgcn_make_buffer_rsrc((void*)x, 0, w == 7 ? 8 * 1024 * 2 : 0, 0x00020000);
    const int hd = r & 31, ks2 = r >> 5;
    const int ob = r >> 4, ocb = r & 15;
    const int ocol = ob * 1024 + ocb * 64 + 32 * (m >> 3) + (m & 7);
    const v2u ev = __builtin_amdgcn_raw_buffer_load_b64(rsW, 0, 0, 16);
    const v4u xa = LDPL(rsX, ob * 2048 + l * 32, 0);
    const v4u xb = LDPL(rsX, ob * 2048 + l * 32 + 16, 0);
    const u32 xs0 = __builtin_amdgcn_raw_buffer_load_b16(rsX, (ocol + 8 * (g & 1)) * 2, 0, 0);
    const u32 xs1 = __builtin_amdgcn_raw_buffer_load_b16(rsX, (ocol + 8 * (g & 1) + 16) * 2, 0, 0);
    u32 xo = __builtin_amdgcn_raw_buffer_load_b16(rsX, (ocol + 8 * prow) * 2, 0, 0);
    Unit U;
    load_rm(U, rsQ, 2 * g * 8192 + m * 16, (ks2 * 256 + 32 * w) * 8192 + hd * 256, 8192);
    make_tag(ev);
    if (w == 7) {
      float s = 0.f;
#pragma unroll
      for (int k = 0; k < 4; ++k) s += bflo(xa[k]) + bfhi(xa[k]) + bflo(xb[k]) + bfhi(xb[k]);
      const float mu = sum64(s) * (1.f / 1024.f);
      float v = 0.f;
#pragma unroll
      for (int k = 0; k < 4; ++k) {
        float d;
        d = bflo(xa[k]) - mu; v += d * d;
        d = bfhi(xa[k]) - mu; v += d * d;
        d = bflo(xb[k]) - mu; v += d * d;
        d = bfhi(xb[k]) - mu; v += d * d;
      }
      v = rbf(sum64(v) * (1.f / 1024.f));
      const float mean1 = rbf(mu), den1 = rbf(sqrtf(rbf(v + 1e-5f)));
      float xn0 = rbf(rbf(bflo(xs0) - mean1) / den1), xn1 = rbf(rbf(bflo(xs1) - mean1) / den1);
      PIN(xn0); PIN(xn1);
      const int poff = OFF_P1 + r * 4096 + g * 512 + m * 16;
      v4u d0, d1, d2, d3;
      STAMP(1);
      POLL((d0 = LDSC(rsW, poff, 0), d1 = LDSC(rsW, poff + 256, 0), d2 = LDSC(rsW, poff + 2048, 0), d3 = LDSC(rsW, poff + 2304, 0)),
           bad4(d0, d1, T) | bad4(d2, d3, T));
      STAMP(2);
      const v4f sa = dec4acc(d1, dec4(d0)), ca = dec4acc(d3, dec4(d2));
      const u32 p = pack2(sadd16(sadd32(sa.x, ca.x), sadd32(sa.y, ca.y)), sadd16(sadd32(sa.z, ca.z), sadd32(sa.w, ca.w)));
      const v2u sc = __builtin_amdgcn_permlane32_swap(p, p, false, false);
      const float h0 = rbf(xn0 * rbf(1.f + bflo(sc.y))) + bflo(sc.x), h1 = rbf(xn1 * rbf(1.f + bfhi(sc.y))) + bfhi(sc.x);
      const u32 ph = pack2(h0, h1);
      const v2u hp = __builtin_amdgcn_permlane16_swap(ph, ph, false, false);
      if (l < 16) STSC((v4u{hp.x, hp.y, T.a, T.b}), rsW, OFF_H + ob * 4096 + (ocb * 16 + m) * 16, 0);
      STAMP(3);
    }
    transpose_unit(U);
    {
      const int hoff = OFF_H + (m & 7) * 4096 + ((ks2 * 8 + w) * 8 + 2 * g + (m >> 3)) * 16;
      v4u hg;
      POLL(hg = LDSC(rsW, hoff, 0), bad2(hg, T));
      STAMP(4);
      v4f acc[8];
      mma_first(acc, U, v4u{hg.x, hg.y, dppu<0x128>(hg.x), dppu<0x128>(hg.y)});
      store_red(smem + L_RA + w * RBLK, acc, m, g);
      BAR();
      if (tid < 256) {
        const int b = tid >> 5;
        v4f s = *(const v4f*)(smem + L_RA + b * RROW + rq * 16);
#pragma unroll
        for (int k = 1; k < 8; ++k) s += *(const v4f*)(smem + L_RA + k * RBLK + b * RROW + rq * 16);
        STSC(enc4(s, T), rsW, OFF_Q + (b * 4 + (hd >> 3)) * 16384 + (hd & 7) * 2048 + ks2 * 512 + rq * 16, 0);
      }
      BAR();
    }
    STAMP(5);
    const int hd4 = r >> 2, nq = r & 3;
    Unit UO;
    {
      const int ksm = r >> 4, nsm = r & 15;
      const v2u c8 = __builtin_amdgcn_raw_buffer_load_b64(rsC, (m & 7) * 2048 + (ksm * 128 + (w >> 1) * 32 + 8 * g + 4 * (m >> 3)) * 2, 0, 0);
      load_nat(U, rsM, 8 * g * 12288 + m * 16, (ksm * 128 + (w >> 1) * 32) * 12288 + 4096 + nsm * 512 + (w & 1) * 256, 12288);
      load_rm(UO, rsO, 2 * g * 2048 + m * 16, (hd4 * 128 + (w >> 1) * 32) * 2048 + nq * 512 + (w & 1) * 256, 2048);
      const v4u cf = silu_frag(c8);
      transpose_unit(U);
      v4f acc[8];
      mma_first(acc, U, cf);
      store_red(smem + L_RB + w * RBLK, acc, m, g);
      BAR();
      v4f s = *(const v4f*)(smem + L_RB + rch * RBLK + rb * RROW + rq * 16);
#pragma unroll
      for (int k = 1; k < 4; ++k) s += *(const v4f*)(smem + L_RB + (k * 2 + rch) * RBLK + rb * RROW + rq * 16);
      const int which = nsm >> 2;
      const int off = which < 3 ? OFF_MOD + (rb * 16 + (nsm & 3) * 4 + rch * 2 + (rq >> 4)) * 6144 + which * 2048 + ksm * 256 + (rq & 15) * 16
                                : OFF_GB + (rb * 32 + (nsm & 3) * 8 + rch * 4 + (rq >> 3)) * 1024 + ksm * 128 + (rq & 7) * 16;
      STSC(enc4(s, T), rsW, off, 0);
    }
    STAMP(6);
    transpose_unit(UO);
    {
      const int ooff = OFF_O + (hd4 * 8 + w) * 2048 + l * 16;
      const int soff = OFF_OS + ((hd4 * 8 + w) * 4 + (l & 3)) * 16;
      v4u d0, d1, sv;
      POLL((sv = LDSC(rsW, soff, 0), d0 = LDSC(rsW, ooff, 0), d1 = LDSC(rsW, ooff + 1024, 0)), bad2(sv, T) | bad4(d0, d1, T));
      STAMP(7);
      const float Ms = asf(sv.x), Ls = asf(sv.y);
      float M = fmaxf(Ms, dpp<0xB1>(Ms));
      M = fmaxf(M, dpp<0x4E>(M));
      const float es = __expf(Ms - M);
      float den = es * Ls;
      den += dpp<0xB1>(den);
      den += dpp<0x4E>(den);
      const float fs = es * __builtin_amdgcn_rcpf(den);
      const float f0 = dpp<0x00>(fs), f1 = dpp<0x55>(fs), f2 = dpp<0xAA>(fs), f3 = dpp<0xFF>(fs);
      const float fa = (l < 32 ? f0 : f1) * gscale(d0), fb = (l < 32 ? f2 : f3) * gscale(d1);
      float o4[4];
#pragma unroll
      for (int k = 0; k < 4; ++k) o4[k] = fmaf((float)((int)d1[k] >> 10), fb, (float)((int)d0[k] >> 10) * fa);
      *(u32*)(smem + L_SA + w * RROW + rq * 8 + (l >> 5) * 4) = pack2(sadd32(o4[0], o4[1]), sadd32(o4[2], o4[3]));
      BAR();
      const v4u a = *(const v4u*)(smem + L_SA + (m & 7) * RROW + (w >> 1) * 64 + g * 16);
      v4f acc[8];
      mma_first(acc, UO, a);
      store_red(smem + L_RA + w * RBLK, acc, m, g);
      BAR();
      v4f s = *(const v4f*)(smem + L_RA + rch * RBLK + rb * RROW + rq * 16);
#pragma unroll
      for (int k = 1; k < 4; ++k) s += *(const v4f*)(smem + L_RA + (k * 2 + rch) * RBLK + rb * RROW + rq * 16);
      STSC(enc4(s, T), rsW, OFF_WO + (rb * 16 + nq * 4 + rch * 2 + (rq >> 4)) * 8192 + hd4 * 256 + (rq & 15) * 16, 0);
    }
    STAMP(8);
    float ga = 0.f;
    {
      v4u d;
      POLL(d = LDSC(rsW, OFF_MOD + r * 6144 + (w < 6 ? w : 0) * 1024 + l * 16, 0), bad4(d, T));
      *(float*)(smem + L_R2 + l * 48 + w * 4) = fold4(dec4(d));
      BAR();
      if (w == 7) {
        const v4f s0 = *(const v4f*)(smem + L_R2 + l * 48);
        const v2f s1 = *(const v2f*)(smem + L_R2 + l * 48 + 16);
        ga = rbf(s0.x + s0.y);
        const u32 pu = pack2(1.f + rbf(s1.x + s1.y), s0.z + s0.w);
        const v2u a = __builtin_amdgcn_permlane16_swap(pu, pu, false, false);
        const u32 lu = __builtin_amdgcn_perm(a.y, a.x, 0x05040100u), ls = __builtin_amdgcn_perm(a.y, a.x, 0x07060302u);
        const v2u b = __builtin_amdgcn_permlane32_swap(lu, ls, false, false);
        if (g == 0 || g == 2)
          STSC((v4u{b.x, b.y, T.a, T.b}), rsW, OFF_US + ((ob * 256 + ocb * 16 + m) * 2 + (g >> 1)) * 16, 0);
      }
    }
    {
      v4u d;
      POLL(d = LDSC(rsW, OFF_WO + r * 8192 + w * 1024 + l * 16, 0), bad4(d, T));
      STAMP(9);
      *(float*)(smem + L_R2 + 3072 + l * 48 + w * 4) = fold4(dec4(d));
      BAR();
      if (w == 7) {
        const v4f s0 = *(const v4f*)(smem + L_R2 + 3072 + l * 48), s1 = *(const v4f*)(smem + L_R2 + 3072 + l * 48 + 16);
        const float attn = ((s0.x + s0.y) + (s0.z + s0.w)) + ((s1.x + s1.y) + (s1.z + s1.w));
        PIN(xo);
        const float x1 = rbf(bflo(xo) + rbf(ga * rbf(attn)));
        const u32 pz = asu(x1) >> 16;
        const v2u a = __builtin_amdgcn_permlane16_swap(pz, pz, false, false);
        const u32 lo = a.x | (a.y << 16);
        const v2u p = __builtin_amdgcn_permlane32_swap(lo, lo, false, false);
        if (l < 16) STSC((v4u{p.x, p.y, T.a, T.b}), rsW, OFF_X1 + ob * 4096 + (ocb * 16 + m) * 16, 0);
        float t = sadd32(x1, x1 * x1);
        t = sum16(sadd16(t, t));
        const v2u sq = __builtin_amdgcn_permlane32_swap(asu(t), asu(t), false, false);
        if (l == 0) STSC((v4u{sq.x, sq.y, T.a, T.b}), rsW, OFF_ST + r * 16, 0);
      }
      BAR();
    }
    STAMP(10);
    if (r < 112) {
      const rsrc_t rsD = __builtin_amdgcn_make_buffer_rsrc((void*)wd, 0, 3584 * 1024 * 2, 0x00020000);
      const int k6 = r >> 1, nh = r & 1;
      load_rm(U, rsD, 2 * g * 2048 + m * 16, (k6 * 64 + (w & 1) * 32) * 2048 + nh * 1024 + (w >> 1) * 256, 2048);
      transpose_unit(U);
      const int goff = OFF_GU + (k6 * 8 + w) * 2048 + l * 16;
      v4u d0, d1;
      POLL((d0 = LDSC(rsW, goff, 0), d1 = LDSC(rsW, goff + 1024, 0)), bad4(d0, d1, T));
      STAMP(11);
      const v4f gt = dec4(d0), up = dec4(d1);
      const u32 p = pack2(sadd16(sadd32(gt.x, up.x), sadd32(gt.y, up.y)), sadd16(sadd32(gt.z, up.z), sadd32(gt.w, up.w)));
      const v2u s = __builtin_amdgcn_permlane32_swap(p, p, false, false);
      const float g0 = bflo(s.x), g1 = bfhi(s.x);
      const float a0 = rbf(g0 * __builtin_amdgcn_rcpf(1.f + __expf(-g0))) * bflo(s.y);
      const float a1 = rbf(g1 * __builtin_amdgcn_rcpf(1.f + __expf(-g1))) * bfhi(s.y);
      if (l < 32) *(u32*)(smem + L_SA + w * RROW + m * 8 + g * 4) = pack2(a0, a1);
      BAR();
      const v4u a = *(const v4u*)(smem + L_SA + (m & 7) * RROW + (w & 1) * 64 + g * 16);
      v4f acc[8];
      mma_first(acc, U, a);
      store_red(smem + L_RB + w * RBLK, acc, m, g);
      BAR();
#pragma unroll
      for (int hh = 0; hh < 2; ++hh) {
        const int cq = hh * 2 + rch;
        const v4f s2 = *(const v4f*)(smem + L_RB + (2 * cq) * RBLK + rb * RROW + rq * 16) +
                       *(const v4f*)(smem + L_RB + (2 * cq + 1) * RBLK + rb * RROW + rq * 16);
        STSC(enc4(s2, T), rsW, OFF_DN + (rb * 32 + nh * 16 + cq * 4 + (rq >> 3)) * 7168 + k6 * 128 + (rq & 7) * 16, 0);
      }
    }
    STAMP(12);
  } else {
    const rsrc_t rsK = __builtin_amdgcn_make_buffer_rsrc((void*)kc, 0, 8 * 4 * 4096 * 128 * 2, 0x00020000);
    const rsrc_t rsV = __builtin_amdgcn_make_buffer_rsrc((void*)vc, 0, 8 * 4 * 4096 * 128 * 2, 0x00020000);
    const v2u ev = __builtin_amdgcn_raw_buffer_load_b64(rsW, 0, 0, 16);
    const int ks1 = r >> 4, ns1 = r & 15;
    if (w < 4) {
      Unit PU;
      const v2u c8 = __builtin_amdgcn_raw_buffer_load_b64(rsC, (m & 7) * 2048 + (ks1 * 128 + w * 32 + 8 * g + 4 * (m >> 3)) * 2, 0, 0);
      load_nat(PU, rsM, 8 * g * 12288 + m * 16, (ks1 * 128 + w * 32) * 12288 + ns1 * 256, 12288);
      const v4u cf = silu_frag(c8);
      transpose_unit(PU);
      v4f acc[8];
      mma_first(acc, PU, cf);
      store_red(smem + L_RA + w * RBLK, acc, m, g);
    }
    make_tag(ev);
    BAR();
    if (w < 4) {
      const int b = tid >> 5;
      v4f s = *(const v4f*)(smem + L_RA + b * RROW + rq * 16);
#pragma unroll
      for (int k = 1; k < 4; ++k) s += *(const v4f*)(smem + L_RA + k * RBLK + b * RROW + rq * 16);
      STSC(enc4(s, T), rsW, OFF_P1 + (b * 16 + (ns1 & 7) * 2 + (rq >> 4)) * 4096 + (ns1 >> 3) * 2048 + ks1 * 256 + (rq & 15) * 16, 0);
    }
    BAR();
    STAMP(1);
    const int ab = r >> 4, g4 = (r >> 2) & 3, sp4 = r & 3;
    const int kvoff = ((ab * 4 + g4) * 4096 + 3072 + sp4 * 256 + 32 * w) * 256;
    Unit KU, VU;
    load_nat(KU, rsK, l * 16, kvoff, 1024);
    load_nat(VU, rsV, l * 16, kvoff, 1024);
    {
      unsigned char* ks = smem + L_KS + w * 8192;
#pragma unroll
      for (int i = 0; i < 8; ++i) {
        const int key = 4 * i + g;
        *(v4u*)(ks + key * 256 + ((m ^ (key & 15)) * 16)) = KU.a[i];
      }
      asm volatile("s_waitcnt lgkmcnt(0)" ::: "memory");
#pragma unroll
      for (int t = 0; t < 2; ++t) {
        const int key = 16 * t + 4 * (m & 3) + (m >> 2);
#pragma unroll
        for (int kk = 0; kk < 4; ++kk) KU.a[t * 4 + kk] = *(const v4u*)(ks + key * 256 + (((4 * kk + g) ^ (key & 15)) * 16));
      }
#pragma unroll
      for (int i = 0; i < 8; ++i) PIN(KU.a[i]);
    }
    transpose_unit(VU);
    STAMP(2);
    {
      const int qoff = OFF_Q + (ab * 4 + g4) * 16384 + w * 2048 + l * 16;
      v4u d0, d1;
      POLL((d0 = LDSC(rsW, qoff, 0), d1 = LDSC(rsW, qoff + 1024, 0)), bad4(d0, d1, T));
      STAMP(4);
      const v4f a = dec4acc(d1, dec4(d0));
      const u32 p = pack2(sadd32(a.x, a.y), sadd32(a.z, a.w));
      unsigned char* qp = smem + L_SA + w * RROW + (32 * (rq >> 3) + (rq & 7) + 8 * (l >> 5)) * 2;
      *(u16*)qp = (u16)p;
      *(u16*)(qp + 32) = (u16)(p >> 16);
      BAR();
      v4f s0 = v4f{0.f, 0.f, 0.f, 0.f}, s1 = v4f{0.f, 0.f, 0.f, 0.f};
#pragma unroll
      for (int kk = 0; kk < 4; ++kk) {
        const v4u qf = *(const v4u*)(smem + L_SA + (m & 7) * RROW + (32 * kk + 8 * g) * 2);
        s0 = mfma(KU.a[kk], qf, s0);
        s1 = mfma(KU.a[4 + kk], qf, s1);
      }
      float sc[8];
#pragma unroll
      for (int i = 0; i < 4; i += 2) {
        const u32 pa = pack2(s0[i], s0[i + 1]), pb = pack2(s1[i], s1[i + 1]);
        const u32 qa = pack2(bflo(pa) * 0.08838834764831845f, bfhi(pa) * 0.08838834764831845f);
        const u32 qb = pack2(bflo(pb) * 0.08838834764831845f, bfhi(pb) * 0.08838834764831845f);
        sc[i] = bflo(qa); sc[i + 1] = bfhi(qa); sc[4 + i] = bflo(qb); sc[5 + i] = bfhi(qb);
      }
      float mx = fmaxf(fmaxf(fmaxf(sc[0], sc[1]), sc[2]), fmaxf(fmaxf(sc[3], sc[4]), sc[5]));
      mx = fmaxf(fmaxf(mx, sc[6]), sc[7]);
      mx = max32(max16(mx));
      if (l < 8) *(float*)(smem + L_R2 + l * 32 + w * 4) = mx;
      BAR();
      const v4f m0 = *(const v4f*)(smem + L_R2 + (m & 7) * 32), m1 = *(const v4f*)(smem + L_R2 + (m & 7) * 32 + 16);
      const float M = fmaxf(fmaxf(fmaxf(fmaxf(m0.x, m0.y), m0.z), fmaxf(fmaxf(m0.w, m1.x), m1.y)), fmaxf(m1.z, m1.w));
      const float nM = -M * 1.44269504088896341f;
#pragma unroll
      for (int i = 0; i < 8; ++i) sc[i] = __builtin_amdgcn_exp2f(fmaf(sc[i], 1.44269504088896341f, nM));
      const v4u pf = v4u{pack2(sc[0], sc[1]), pack2(sc[2], sc[3]), pack2(sc[4], sc[5]), pack2(sc[6], sc[7])};
      v4f acc[8];
      mma_first(acc, VU, pf);
      const v4f ls = mfma(v4u{0x3F803F80u, 0x3F803F80u, 0x3F803F80u, 0x3F803F80u}, pf, v4f{0.f, 0.f, 0.f, 0.f});
      store_red(smem + L_RB + w * RBLK, acc, m, g);
      if (l < 8) *(float*)(smem + L_R2 + 512 + l * 32 + w * 4) = ls.x;
      BAR();
      if (w < 4) {
        const int hh = tid >> 5;
        v4f o = *(const v4f*)(smem + L_RB + hh * RROW + rq * 16);
#pragma unroll
        for (int k = 1; k < 8; ++k) o += *(const v4f*)(smem + L_RB + k * RBLK + hh * RROW + rq * 16);
        STSC(enc4(o, T), rsW, OFF_O + (((g4 * 8 + hh) * 8 + ab) * 4 + sp4) * 512 + rq * 16, 0);
      } else if (w == 4) {
        const v4f l0 = *(const v4f*)(smem + L_R2 + 512 + (m & 7) * 32), l1 = *(const v4f*)(smem + L_R2 + 512 + (m & 7) * 32 + 16);
        const float L = ((l0.x + l0.y) + (l0.z + l0.w)) + ((l1.x + l1.y) + (l1.z + l1.w));
        if (l < 8) STSC((v4u{asu(M), asu(L), T.a, T.b}), rsW, OFF_OS + (((g4 * 8 + l) * 8 + ab) * 4 + sp4) * 16, 0);
      }
      BAR();
    }
    STAMP(5);
    if (r < 112) {
      const int mat = r & 1, t5 = r >> 1, ns5 = t5 % 14, ks5 = t5 / 14;
      const rsrc_t rsG = __builtin_amdgcn_make_buffer_rsrc((void*)(mat ? wu : wg), 0, 1024 * 3584 * 2, 0x00020000);
      const int so = (ks5 * 256 + (w >> 1) * 64) * 7168 + ns5 * 512 + (w & 1) * 256;
      load_rm(KU, rsG, 2 * g * 7168 + m * 16, so, 7168);
      load_rm(VU, rsG, 2 * g * 7168 + m * 16, so + 32 * 7168, 7168);
      transpose_unit(KU);
      transpose_unit(VU);
      STAMP(6);
      const int gi = w * 256 + ks5 * 64 + l;
      v4u du, ds;
      POLL((du = LDSC(rsW, OFF_US + gi * 32, 0), ds = LDSC(rsW, OFF_US + gi * 32 + 16, 0)), bad2(du, T) | bad2(ds, T));
      float uu[4] = {bflo(du.x), bfhi(du.x), bflo(du.y), bfhi(du.y)}, sb[4] = {bflo(ds.x), bfhi(ds.x), bflo(ds.y), bfhi(ds.y)};
#pragma unroll
      for (int k = 0; k < 4; ++k) { PIN(uu[k]); PIN(sb[k]); }
      STAMP(7);
      const int soff = OFF_ST + (w * 16 + m) * 16;
      v4u d0, sv;
      POLL((sv = LDSC(rsW, soff, 0), d0 = LDSC(rsW, OFF_X1 + gi * 16, 0)), bad2(sv, T) | bad2(d0, T));
      STAMP(8);
      const float mu = sum16(asf(sv.x)) * (1.f / 1024.f);
      const float var = rbf(fmaxf(sum16(asf(sv.y)) * (1.f / 1024.f) - mu * mu, 0.f));
      const float mb = rbf(mu);
      const float rden = __builtin_amdgcn_rcpf(rbf(__builtin_amdgcn_sqrtf(rbf(var + 1e-5f))));
      u32 hp[2];
#pragma unroll
      for (int t = 0; t < 2; ++t) {
        const u32 xd = t ? d0.y : d0.x;
        const u32 a = pack2(bflo(xd) - mb, bfhi(xd) - mb);
        const u32 b = pack2(bflo(a) * rden, bfhi(a) * rden);
        const u32 e = pack2(bflo(b) * uu[2 * t], bfhi(b) * uu[2 * t + 1]);
        hp[t] = pack2(bflo(e) + sb[2 * t], bfhi(e) + sb[2 * t + 1]);
      }
      *(v2u*)(smem + L_SA + w * RROW + l * 8) = v2u{hp[0], hp[1]};
      BAR();
      const v4u a0 = *(const v4u*)(smem + L_SA + (m & 7) * RROW + (w >> 1) * 128 + g * 16);
      const v4u a1 = *(const v4u*)(smem + L_SA + (m & 7) * RROW + (w >> 1) * 128 + 64 + g * 16);
      v4f acc[8];
      mma_first(acc, KU, a0);
      mma_next(acc, VU, a1);
      store_red(smem + L_RA + w * RBLK, acc, m, g);
      BAR();
      v4f s = *(const v4f*)(smem + L_RA + rch * RBLK + rb * RROW + rq * 16);
#pragma unroll
      for (int k = 1; k < 4; ++k) s += *(const v4f*)(smem + L_RA + (k * 2 + rch) * RBLK + rb * RROW + rq * 16);
      STSC(enc4(s, T), rsW, OFF_GU + (((ns5 * 4 + rch * 2 + (rq >> 4)) * 8 + rb) * 8 + mat * 4 + ks5) * 256 + (rq & 15) * 16, 0);
    }
    STAMP(12);
  }

  {
    const int ob = u >> 5, c32 = u & 31;
    float gbv = 0.f, x1v = 0.f;
    if (w == 7) {
      v4u d0, d1;
      POLL((d0 = LDSC(rsW, OFF_GB + u * 1024 + l * 16, 0), d1 = LDSC(rsW, OFF_X1 + ob * 4096 + (c32 * 8 + (l & 7)) * 16, 0)),
           bad4(d0, T) | bad2(d1, T));
      float z = fold4(dec4(d0));
      z += dpp<0x128>(z);
      gbv = rbf(z);
      const u32 xd = g < 2 ? d1.x : d1.y;
      x1v = (g & 1) ? bfhi(xd) : bflo(xd);
    } else {
      v4u d0;
      POLL(d0 = LDSC(rsW, OFF_DN + u * 7168 + w * 1024 + l * 16, 0), bad4(d0, T));
      STAMP(13);
      float z = fold4(dec4(d0));
      z += dpp<0x128>(z);
      *(float*)(smem + L_R2 + 6144 + (g * 8 + (l & 7)) * 32 + w * 4) = z;
    }
    BAR();
    if (w == 7) {
      const v4f s0 = *(const v4f*)(smem + L_R2 + 6144 + (g * 8 + (l & 7)) * 32), s1 = *(const v4f*)(smem + L_R2 + 6144 + (g * 8 + (l & 7)) * 32 + 16);
      const float f = ((s0.x + s0.y) + (s0.z + s0.w)) + ((s1.x + s1.y) + s1.z);
      const float o = x1v + rbf(gbv * rbf(f));
      if ((l & 8) == 0) out[ob * 1024 + c32 * 32 + (l & 7) + 8 * prow] = (u16)(pack2(o, 0.f) & 0xFFFFu);
      if (u == 0 && l == 0) __builtin_amdgcn_raw_buffer_store_b64(v2u{elo, ehi}, rsW, 0, 0, 16);
    }
  }
}

extern "C" void kernel_launch(void* const* d_in, const int* in_sizes, int n_in, void* d_out, int out_size, void* d_ws,
                              size_t ws_size, hipStream_t stream) {
  fused_block<<<256, 512, 0, stream>>>((const u16*)d_in[0], (const u16*)d_in[1], (const u16*)d_in[2], (const u16*)d_in[3],
                                       (const u16*)d_in[4], (const u16*)d_in[6], (const u16*)d_in[7], (const u16*)d_in[8],
                                       (const u16*)d_in[9], (const u16*)d_in[10], (u16*)d_out, (unsigned char*)d_ws);
}
```

```cpp
#define HOLD 0
#define HOLDC(c) (c==128)
#include <hip/hip_runtime.h>
#include <stdint.h>

typedef unsigned u32;
typedef unsigned long long u64;
typedef unsigned short u16;
typedef u32 v4u __attribute__((ext_vector_type(4)));
typedef u32 v2u __attribute__((ext_vector_type(2)));
typedef float v4f __attribute__((ext_vector_type(4)));
typedef float v2f __attribute__((ext_vector_type(2)));
typedef __bf16 v8b __attribute__((ext_vector_type(8)));
typedef __bf16 v2b __attribute__((ext_vector_type(2)));
typedef __amdgpu_buffer_rsrc_t rsrc_t;
#define DEVI static __device__ __forceinline__

constexpr int OFF_P1 = 0x001000;
constexpr int OFF_H = 0x081000;
constexpr int OFF_Q = 0x089000;
constexpr int OFF_O = 0x109000;
constexpr int OFF_OS = 0x189000;
constexpr int OFF_MOD = 0x18D000;
constexpr int OFF_GB = 0x24D000;
constexpr int OFF_WO = 0x28D000;
constexpr int OFF_X1 = 0x38D000;
constexpr int OFF_US = 0x395000;
constexpr int OFF_ST = 0x3A5000;
constexpr int OFF_GU = 0x3A6000;
constexpr int OFF_DN = 0x486000;
constexpr int OFF_DBG = 0x800000;
constexpr int WS_USED = 0x900000;

constexpr int RROW = 528;
constexpr int RBLK = 8 * RROW;
constexpr int L_KS = 0;
constexpr int L_RA = 65536;
constexpr int L_RB = L_RA + 8 * RBLK;
constexpr int L_SA = L_RB + 8 * RBLK;
constexpr int L_R2 = L_SA + RBLK;
constexpr int L_TOTAL = L_R2 + 8192;
constexpr unsigned SPIN_MAX = 200000;

#define LDNT(rs, vo, so) __builtin_amdgcn_raw_buffer_load_b128(rs, vo, so, 2)
#define LDPL(rs, vo, so) __builtin_amdgcn_raw_buffer_load_b128(rs, vo, so, 0)
#define LDSC(rs, vo, so) __builtin_amdgcn_raw_buffer_load_b128(rs, vo, so, 16)
#define STSC(v, rs, vo, so) __builtin_amdgcn_raw_buffer_store_b128(v, rs, vo, so, 16)
#define BAR() asm volatile("s_waitcnt lgkmcnt(0)\n\ts_barrier" ::: "memory")
#define FENCE() asm volatile("" ::: "memory")
#define PIN(x) asm volatile("" : "+v"(x))

DEVI float asf(u32 x) { return __uint_as_float(x); }
DEVI u32 asu(float x) { return __float_as_uint(x); }
DEVI float bflo(u32 x) { return asf(x << 16); }
DEVI float bfhi(u32 x) { return asf(x & 0xFFFF0000u); }
DEVI u32 pack2(float lo, float hi) {
  v2b p = {(__bf16)lo, (__bf16)hi};
  return __builtin_bit_cast(u32, p);
}
DEVI float rbf(float f) { return (float)(__bf16)f; }
DEVI float silu_div(float v) { return v / (1.f + __expf(-v)); }

template <int CTRL> DEVI float dpp(float x) { return asf(__builtin_amdgcn_update_dpp(0u, asu(x), CTRL, 0xf, 0xf, false)); }
template <int CTRL> DEVI u32 dppu(u32 x) { return __builtin_amdgcn_update_dpp(0u, x, CTRL, 0xf, 0xf, false); }
DEVI float sum16(float x) { x += dpp<0x128>(x); x += dpp<0x124>(x); x += dpp<0x122>(x); x += dpp<0x121>(x); return x; }
DEVI float sadd32(float a, float b) { v2u s = __builtin_amdgcn_permlane32_swap(asu(a), asu(b), false, false); return asf(s.x) + asf(s.y); }
DEVI float sadd16(float a, float b) { v2u s = __builtin_amdgcn_permlane16_swap(asu(a), asu(b), false, false); return asf(s.x) + asf(s.y); }
DEVI float max16(float x) { v2u a = __builtin_amdgcn_permlane16_swap(asu(x), asu(x), false, false); return fmaxf(asf(a.x), asf(a.y)); }
DEVI float max32(float x) { v2u a = __builtin_amdgcn_permlane32_swap(asu(x), asu(x), false, false); return fmaxf(asf(a.x), asf(a.y)); }
DEVI float sum64(float x) { x = sum16(x); x = sadd16(x, x); return sadd32(x, x); }
DEVI bool any_bad(u32 bad) { return __builtin_amdgcn_ballot_w64(bad != 0u) != 0ull; }

struct Tag { u32 t0, t1, t2, t3, e0, a, b; };
DEVI v4u enc4(v4f v, const Tag& T) {
  float mx = fmaxf(fmaxf(fabsf(v.x), fabsf(v.y)), fabsf(v.z));
  mx = fmaxf(fmaxf(mx, fabsf(v.w)), 0x1p-106f) * 1.00000095367431640625f;
  const u32 eb = asu(mx) & 0x7F800000u;
  const float inv = asf(0x89000000u - eb);
  const float mg = 12582912.f;
  return v4u{(asu(fmaf(v.x, inv, mg)) << 10) | ((eb >> 23) + T.e0), (asu(fmaf(v.y, inv, mg)) << 10) | T.t1,
             (asu(fmaf(v.z, inv, mg)) << 10) | T.t2, (asu(fmaf(v.w, inv, mg)) << 10) | T.t3};
}
DEVI u32 bad3(v4u g, const Tag& T) { return (g.y ^ T.t1) | (g.z ^ T.t2) | (g.w ^ T.t3); }
DEVI u32 badf(u32 y, u32 z) { return (y & 0x3FFu) | (z & 0x300u); }
DEVI u32 bad4(v4u g, const Tag& T) { return badf(bad3(g, T), g.x ^ T.t0); }
DEVI u32 bad4(v4u g, v4u h, const Tag& T) { return badf(bad3(g, T) | bad3(h, T), (g.x ^ T.t0) | (h.x ^ T.t0)); }
DEVI u32 bad2(v4u g, const Tag& T) { return (g.z ^ T.a) | (g.w ^ T.b); }
DEVI float gscale(v4u g) { return fabsf(asf(g.x << 23)); }
DEVI v4f dec4(v4u g) {
  const float s = gscale(g);
  return v4f{(float)((int)g.x >> 10) * s, (float)((int)g.y >> 10) * s, (float)((int)g.z >> 10) * s, (float)((int)g.w >> 10) * s};
}
DEVI v4f dec4acc(v4u g, v4f a) {
  const float s = gscale(g);
  return v4f{fmaf((float)((int)g.x >> 10), s, a.x), fmaf((float)((int)g.y >> 10), s, a.y), fmaf((float)((int)g.z >> 10), s, a.z),
             fmaf((float)((int)g.w >> 10), s, a.w)};
}
DEVI float fold4(v4f v) { return sadd16(sadd32(v.x, v.y), sadd32(v.z, v.w)); }

struct Unit { v4u a[8]; };
DEVI void load_nat(Unit& U, rsrc_t rs, int voff, int soff, int stride) {
#pragma unroll
  for (int j = 0; j < 8; ++j) U.a[j] = LDNT(rs, voff, soff + j * stride);
}
DEVI void load_rm(Unit& U, rsrc_t rs, int voff, int soff, int stride) {
#pragma unroll
  for (int j = 0; j < 8; ++j) U.a[j] = LDNT(rs, voff, soff + ((j >> 2) + 8 * (((j & 1) << 1) | ((j >> 1) & 1))) * stride);
}
DEVI void transpose_unit(Unit& U) {
  Unit T;
#pragma unroll
  for (int i = 0; i < 8; ++i) {
    const u32 sel = (i & 1) ? 0x07060302u : 0x05040100u;
#pragma unroll
    for (int p = 0; p < 4; ++p) T.a[i][p] = __builtin_amdgcn_perm(U.a[2 * p + 1][i >> 1], U.a[2 * p][i >> 1], sel);
  }
#pragma unroll
  for (int i = 0; i < 8; ++i) { U.a[i] = T.a[i]; PIN(U.a[i]); }
}
DEVI v4f mfma(v4u a, v4u b, v4f c) {
  return __builtin_amdgcn_mfma_f32_16x16x32_bf16(__builtin_bit_cast(v8b, a), __builtin_bit_cast(v8b, b), c, 0, 0, 0);
}
DEVI void mma_first(v4f (&acc)[8], const Unit& U, v4u act) {
#pragma unroll
  for (int i = 0; i < 8; ++i) acc[i] = mfma(U.a[i], act, v4f{0.f, 0.f, 0.f, 0.f});
}
DEVI void mma_next(v4f (&acc)[8], const Unit& U, v4u act) {
#pragma unroll
  for (int i = 0; i < 8; ++i) acc[i] = mfma(U.a[i], act, acc[i]);
}
DEVI void store_red(unsigned char* blk, const v4f (&acc)[8], int m, int g) {
  if (m < 8) {
#pragma unroll
    for (int i = 0; i < 8; ++i) *(v4f*)(blk + m * RROW + g * 128 + i * 16) = acc[i];
  }
}
DEVI v4u silu_frag(v2u c) {
  const u32 p0 = pack2(silu_div(bflo(c.x)), silu_div(bfhi(c.x))), p1 = pack2(silu_div(bflo(c.y)), silu_div(bfhi(c.y)));
  return v4u{p0, p1, dppu<0x128>(p0), dppu<0x128>(p1)};
}

#define STAMP(i) do { if ((i) == HOLD) hold_t = __builtin_amdgcn_s_memrealtime(); } while (0)
#define POLL(loads, badexpr) \
  for (unsigned sp_ = 0; sp_ < SPIN_MAX; ++sp_) { FENCE(); loads; if (!any_bad(badexpr)) break; }

__global__ void __launch_bounds__(512, 2)
fused_block(const u16* __restrict__ x, const u16* __restrict__ kc, const u16* __restrict__ vc, const u16* __restrict__ wq,
            const u16* __restrict__ wo, const u16* __restrict__ wg, const u16* __restrict__ wu, const u16* __restrict__ wd,
            const u16* __restrict__ cc, const u16* __restrict__ wm, u16* __restrict__ out, unsigned char* __restrict__ ws) {
  __shared__ __attribute__((aligned(16))) unsigned char smem[L_TOTAL];
  const int tid = threadIdx.x, l = tid & 63, w = __builtin_amdgcn_readfirstlane(tid >> 6);
  const int m = l & 15, g = l >> 4;
  const int c = blockIdx.x, xcd = c & 7, jc = c >> 3, role = jc >> 4, r = xcd * 16 + (jc & 15), u = xcd * 32 + jc;
  u64 hold_t = 0;
  STAMP(0);
  const rsrc_t rsW = __builtin_amdgcn_make_buffer_rsrc((void*)ws, 0, WS_USED, 0x00020000);
  const rsrc_t rsM = __builtin_amdgcn_make_buffer_rsrc((void*)wm, 0, 1024 * 6144 * 2, 0x00020000);
  const rsrc_t rsC = __builtin_amdgcn_make_buffer_rsrc((void*)cc, 0, 8 * 1024 * 2, 0x00020000);
  asm volatile("" ::"s"(x), "s"(kc), "s"(vc), "s"(wq), "s"(wo), "s"(wg), "s"(wu), "s"(wd), "s"(cc), "s"(wm), "s"(out), "s"(ws));
  Tag T;
  u32 elo, ehi;
  auto make_tag = [&](v2u ev) {
    const u64 E = (((u64)ev.y << 32) | ev.x) + 1ull;
    elo = __builtin_amdgcn_readfirstlane((u32)E);
    ehi = __builtin_amdgcn_readfirstlane((u32)(E >> 32));
    T.t0 = (elo & 3u) << 8;
    T.t1 = (elo >> 2) & 0x3FFu;
    T.t2 = (elo >> 12) & 0x3FFu;
    T.t3 = elo >> 22;
    T.e0 = T.t0 - 20u;
    T.a = elo;
    T.b = ehi ^ 0x5A5A5A5Au;
  };
  const int rb = tid >> 6, rch = (tid >> 5) & 1, rq = tid & 31;
  const int prow = ((g & 1) << 1) | (g >> 1);

  if (role == 0) {
    const rsrc_t rsQ = __builtin_amdgcn_make_buffer_rsrc((void*)wq, 0, 1024 * 4096 * 2, 0x00020000);
    const rsrc_t rsO = __builtin_amdgcn_make_buffer_rsrc((void*)wo, 0, 4096 * 1024 * 2, 0x00020000);
    const rsrc_t rsX = __builtin_amdgcn_make_buffer_rsrc((void*)x, 0, w == 7 ? 8 * 1024 * 2 : 0, 0x00020000);
    const int hd = r & 31, ks2 = r >> 5;
    const int ob = r >> 4, ocb = r & 15;
    const int ocol = ob * 1024 + ocb * 64 + 32 * (m >> 3) + (m & 7);
    const v2u ev = __builtin_amdgcn_raw_buffer_load_b64(rsW, 0, 0, 16);
    const v4u xa = LDPL(rsX, ob * 2048 + l * 32, 0);
    const v4u xb = LDPL(rsX, ob * 2048 + l * 32 + 16, 0);
    const u32 xs0 = __builtin_amdgcn_raw_buffer_load_b16(rsX, (ocol + 8 * (g & 1)) * 2, 0, 0);
    const u32 xs1 = __builtin_amdgcn_raw_buffer_load_b16(rsX, (ocol + 8 * (g & 1) + 16) * 2, 0, 0);
    u32 xo = __builtin_amdgcn_raw_buffer_load_b16(rsX, (ocol + 8 * prow) * 2, 0, 0);
    Unit U;
    load_rm(U, rsQ, 2 * g * 8192 + m * 16, (ks2 * 256 + 32 * w) * 8192 + hd * 256, 8192);
    make_tag(ev);
    if (w == 7) {
      float s = 0.f;
#pragma unroll
      for (int k = 0; k < 4; ++k) s += bflo(xa[k]) + bfhi(xa[k]) + bflo(xb[k]) + bfhi(xb[k]);
      const float mu = sum64(s) * (1.f / 1024.f);
      float v = 0.f;
#pragma unroll
      for (int k = 0; k < 4; ++k) {
        float d;
        d = bflo(xa[k]) - mu; v += d * d;
        d = bfhi(xa[k]) - mu; v += d * d;
        d = bflo(xb[k]) - mu; v += d * d;
        d = bfhi(xb[k]) - mu; v += d * d;
      }
      v = rbf(sum64(v) * (1.f / 1024.f));
      const float mean1 = rbf(mu), den1 = rbf(sqrtf(rbf(v + 1e-5f)));
      float xn0 = rbf(rbf(bflo(xs0) - mean1) / den1), xn1 = rbf(rbf(bflo(xs1) - mean1) / den1);
      PIN(xn0); PIN(xn1);
      const int poff = OFF_P1 + r * 4096 + g * 512 + m * 16;
      v4u d0, d1, d2, d3;
      STAMP(1);
      POLL((d0 = LDSC(rsW, poff, 0), d1 = LDSC(rsW, poff + 256, 0), d2 = LDSC(rsW, poff + 2048, 0), d3 = LDSC(rsW, poff + 2304, 0)),
           bad4(d0, d1, T) | bad4(d2, d3, T));
      STAMP(2);
      const v4f sa = dec4acc(d1, dec4(d0)), ca = dec4acc(d3, dec4(d2));
      const u32 p = pack2(sadd16(sadd32(sa.x, ca.x), sadd32(sa.y, ca.y)), sadd16(sadd32(sa.z, ca.z), sadd32(sa.w, ca.w)));
      const v2u sc = __builtin_amdgcn_permlane32_swap(p, p, false, false);
      const float h0 = rbf(xn0 * rbf(1.f + bflo(sc.y))) + bflo(sc.x), h1 = rbf(xn1 * rbf(1.f + bfhi(sc.y))) + bfhi(sc.x);
      const u32 ph = pack2(h0, h1);
      const v2u hp = __builtin_amdgcn_permlane16_swap(ph, ph, false, false);
      if (l < 16) STSC((v4u{hp.x, hp.y, T.a, T.b}), rsW, OFF_H + ob * 4096 + (ocb * 16 + m) * 16, 0);
      STAMP(3);
    }
    transpose_unit(U);
    {
      const int hoff = OFF_H + (m & 7) * 4096 + ((ks2 * 8 + w) * 8 + 2 * g + (m >> 3)) * 16;
      v4u hg;
      POLL(hg = LDSC(rsW, hoff, 0), bad2(hg, T));
      STAMP(4);
      v4f acc[8];
      mma_first(acc, U, v4u{hg.x, hg.y, dppu<0x128>(hg.x), dppu<0x128>(hg.y)});
      store_red(smem + L_RA + w * RBLK, acc, m, g);
      BAR();
      if (tid < 256) {
        const int b = tid >> 5;
        v4f s = *(const v4f*)(smem + L_RA + b * RROW + rq * 16);
#pragma unroll
        for (int k = 1; k < 8; ++k) s += *(const v4f*)(smem + L_RA + k * RBLK + b * RROW + rq * 16);
        STSC(enc4(s, T), rsW, OFF_Q + (b * 4 + (hd >> 3)) * 16384 + (hd & 7) * 2048 + ks2 * 512 + rq * 16, 0);
      }
      BAR();
    }
    STAMP(5);
    const int hd4 = r >> 2, nq = r & 3;
    Unit UO;
    {
      const int ksm = r >> 4, nsm = r & 15;
      const v2u c8 = __builtin_amdgcn_raw_buffer_load_b64(rsC, (m & 7) * 2048 + (ksm * 128 + (w >> 1) * 32 + 8 * g + 4 * (m >> 3)) * 2, 0, 0);
      load_nat(U, rsM, 8 * g * 12288 + m * 16, (ksm * 128 + (w >> 1) * 32) * 12288 + 4096 + nsm * 512 + (w & 1) * 256, 12288);
      load_rm(UO, rsO, 2 * g * 2048 + m * 16, (hd4 * 128 + (w >> 1) * 32) * 2048 + nq * 512 + (w & 1) * 256, 2048);
      const v4u cf = silu_frag(c8);
      transpose_unit(U);
      v4f acc[8];
      mma_first(acc, U, cf);
      store_red(smem + L_RB + w * RBLK, acc, m, g);
      BAR();
      v4f s = *(const v4f*)(smem + L_RB + rch * RBLK + rb * RROW + rq * 16);
#pragma unroll
      for (int k = 1; k < 4; ++k) s += *(const v4f*)(smem + L_RB + (k * 2 + rch) * RBLK + rb * RROW + rq * 16);
      const int which = nsm >> 2;
      const int off = which < 3 ? OFF_MOD + (rb * 16 + (nsm & 3) * 4 + rch * 2 + (rq >> 4)) * 6144 + which * 2048 + ksm * 256 + (rq & 15) * 16
                                : OFF_GB + (rb * 32 + (nsm & 3) * 8 + rch * 4 + (rq >> 3)) * 1024 + ksm * 128 + (rq & 7) * 16;
      STSC(enc4(s, T), rsW, off, 0);
    }
    STAMP(6);
    transpose_unit(UO);
    {
      const int ooff = OFF_O + (hd4 * 8 + w) * 2048 + l * 16;
      const int soff = OFF_OS + ((hd4 * 8 + w) * 4 + (l & 3)) * 16;
      v4u d0, d1, sv;
      POLL((sv = LDSC(rsW, soff, 0), d0 = LDSC(rsW, ooff, 0), d1 = LDSC(rsW, ooff + 1024, 0)), bad2(sv, T) | bad4(d0, d1, T));
      STAMP(7);
      const float Ms = asf(sv.x), Ls = asf(sv.y);
      float M = fmaxf(Ms, dpp<0xB1>(Ms));
      M = fmaxf(M, dpp<0x4E>(M));
      const float es = __expf(Ms - M);
      float den = es * Ls;
      den += dpp<0xB1>(den);
      den += dpp<0x4E>(den);
      const float fs = es * __builtin_amdgcn_rcpf(den);
      const float f0 = dpp<0x00>(fs), f1 = dpp<0x55>(fs), f2 = dpp<0xAA>(fs), f3 = dpp<0xFF>(fs);
      const float fa = (l < 32 ? f0 : f1) * gscale(d0), fb = (l < 32 ? f2 : f3) * gscale(d1);
      float o4[4];
#pragma unroll
      for (int k = 0; k < 4; ++k) o4[k] = fmaf((float)((int)d1[k] >> 10), fb, (float)((int)d0[k] >> 10) * fa);
      *(u32*)(smem + L_SA + w * RROW + rq * 8 + (l >> 5) * 4) = pack2(sadd32(o4[0], o4[1]), sadd32(o4[2], o4[3]));
      BAR();
      const v4u a = *(const v4u*)(smem + L_SA + (m & 7) * RROW + (w >> 1) * 64 + g * 16);
      v4f acc[8];
      mma_first(acc, UO, a);
      store_red(smem + L_RA + w * RBLK, acc, m, g);
      BAR();
      v4f s = *(const v4f*)(smem + L_RA + rch * RBLK + rb * RROW + rq * 16);
#pragma unroll
      for (int k = 1; k < 4; ++k) s += *(const v4f*)(smem + L_RA + (k * 2 + rch) * RBLK + rb * RROW + rq * 16);
      STSC(enc4(s, T), rsW, OFF_WO + (rb * 16 + nq * 4 + rch * 2 + (rq >> 4)) * 8192 + hd4 * 256 + (rq & 15) * 16, 0);
    }
    STAMP(8);
    float ga = 0.f;
    {
      v4u d;
      POLL(d = LDSC(rsW, OFF_MOD + r * 6144 + (w < 6 ? w : 0) * 1024 + l * 16, 0), bad4(d, T));
      *(float*)(smem + L_R2 + l * 48 + w * 4) = fold4(dec4(d));
      BAR();
      if (w == 7) {
        const v4f s0 = *(const v4f*)(smem + L_R2 + l * 48);
        const v2f s1 = *(const v2f*)(smem + L_R2 + l * 48 + 16);
        ga = rbf(s0.x + s0.y);
        const u32 pu = pack2(1.f + rbf(s1.x + s1.y), s0.z + s0.w);
        const v2u a = __builtin_amdgcn_permlane16_swap(pu, pu, false, false);
        const u32 lu = __builtin_amdgcn_perm(a.y, a.x, 0x05040100u), ls = __builtin_amdgcn_perm(a.y, a.x, 0x07060302u);
        const v2u b = __builtin_amdgcn_permlane32_swap(lu, ls, false, false);
        if (g == 0 || g == 2)
          STSC((v4u{b.x, b.y, T.a, T.b}), rsW, OFF_US + ((ob * 256 + ocb * 16 + m) * 2 + (g >> 1)) * 16, 0);
      }
    }
    {
      v4u d;
      POLL(d = LDSC(rsW, OFF_WO + r * 8192 + w * 1024 + l * 16, 0), bad4(d, T));
      STAMP(9);
      *(float*)(smem + L_R2 + 3072 + l * 48 + w * 4) = fold4(dec4(d));
      BAR();
      if (w == 7) {
        const v4f s0 = *(const v4f*)(smem + L_R2 + 3072 + l * 48), s1 = *(const v4f*)(smem + L_R2 + 3072 + l * 48 + 16);
        const float attn = ((s0.x + s0.y) + (s0.z + s0.w)) + ((s1.x + s1.y) + (s1.z + s1.w));
        PIN(xo);
        const float x1 = rbf(bflo(xo) + rbf(ga * rbf(attn)));
        const u32 pz = asu(x1) >> 16;
        const v2u a = __builtin_amdgcn_permlane16_swap(pz, pz, false, false);
        const u32 lo = a.x | (a.y << 16);
        const v2u p = __builtin_amdgcn_permlane32_swap(lo, lo, false, false);
        if (l < 16) STSC((v4u{p.x, p.y, T.a, T.b}), rsW, OFF_X1 + ob * 4096 + (ocb * 16 + m) * 16, 0);
        float t = sadd32(x1, x1 * x1);
        t = sum16(sadd16(t, t));
        const v2u sq = __builtin_amdgcn_permlane32_swap(asu(t), asu(t), false, false);
        if (l == 0) STSC((v4u{sq.x, sq.y, T.a, T.b}), rsW, OFF_ST + r * 16, 0);
      }
      BAR();
    }
    STAMP(10);
    if (r < 112) {
      const rsrc_t rsD = __builtin_amdgcn_make_buffer_rsrc((void*)wd, 0, 3584 * 1024 * 2, 0x00020000);
      const int k6 = r >> 1, nh = r & 1;
      load_rm(U, rsD, 2 * g * 2048 + m * 16, (k6 * 64 + (w & 1) * 32) * 2048 + nh * 1024 + (w >> 1) * 256, 2048);
      transpose_unit(U);
      const int goff = OFF_GU + (k6 * 8 + w) * 2048 + l * 16;
      v4u d0, d1;
      POLL((d0 = LDSC(rsW, goff, 0), d1 = LDSC(rsW, goff + 1024, 0)), bad4(d0, d1, T));
      STAMP(11);
      const v4f gt = dec4(d0), up = dec4(d1);
      const u32 p = pack2(sadd16(sadd32(gt.x, up.x), sadd32(gt.y, up.y)), sadd16(sadd32(gt.z, up.z), sadd32(gt.w, up.w)));
      const v2u s = __builtin_amdgcn_permlane32_swap(p, p, false, false);
      const float g0 = bflo(s.x), g1 = bfhi(s.x);
      const float a0 = rbf(g0 * __builtin_amdgcn_rcpf(1.f + __expf(-g0))) * bflo(s.y);
      const float a1 = rbf(g1 * __builtin_amdgcn_rcpf(1.f + __expf(-g1))) * bfhi(s.y);
      if (l < 32) *(u32*)(smem + L_SA + w * RROW + m * 8 + g * 4) = pack2(a0, a1);
      BAR();
      const v4u a = *(const v4u*)(smem + L_SA + (m & 7) * RROW + (w & 1) * 64 + g * 16);
      v4f acc[8];
      mma_first(acc, U, a);
      store_red(smem + L_RB + w * RBLK, acc, m, g);
      BAR();
#pragma unroll
      for (int hh = 0; hh < 2; ++hh) {
        const int cq = hh * 2 + rch;
        const v4f s2 = *(const v4f*)(smem + L_RB + (2 * cq) * RBLK + rb * RROW + rq * 16) +
                       *(const v4f*)(smem + L_RB + (2 * cq + 1) * RBLK + rb * RROW + rq * 16);
        STSC(enc4(s2, T), rsW, OFF_DN + (rb * 32 + nh * 16 + cq * 4 + (rq >> 3)) * 7168 + k6 * 128 + (rq & 7) * 16, 0);
      }
    }
    STAMP(12);
  } else {
    const rsrc_t rsK = __builtin_amdgcn_make_buffer_rsrc((void*)kc, 0, 8 * 4 * 4096 * 128 * 2, 0x00020000);
    const rsrc_t rsV = __builtin_amdgcn_make_buffer_rsrc((void*)vc, 0, 8 * 4 * 4096 * 128 * 2, 0x00020000);
    const v2u ev = __builtin_amdgcn_raw_buffer_load_b64(rsW, 0, 0, 16);
    const int ks1 = r >> 4, ns1 = r & 15;
    if (w < 4) {
      Unit PU;
      const v2u c8 = __builtin_amdgcn_raw_buffer_load_b64(rsC, (m & 7) * 2048 + (ks1 * 128 + w * 32 + 8 * g + 4 * (m >> 3)) * 2, 0, 0);
      load_nat(PU, rsM, 8 * g * 12288 + m * 16, (ks1 * 128 + w * 32) * 12288 + ns1 * 256, 12288);
      const v4u cf = silu_frag(c8);
      transpose_unit(PU);
      v4f acc[8];
      mma_first(acc, PU, cf);
      store_red(smem + L_RA + w * RBLK, acc, m, g);
    }
    make_tag(ev);
    BAR();
    if (w < 4) {
      const int b = tid >> 5;
      v4f s = *(const v4f*)(smem + L_RA + b * RROW + rq * 16);
#pragma unroll
      for (int k = 1; k < 4; ++k) s += *(const v4f*)(smem + L_RA + k * RBLK + b * RROW + rq * 16);
      STSC(enc4(s, T), rsW, OFF_P1 + (b * 16 + (ns1 & 7) * 2 + (rq >> 4)) * 4096 + (ns1 >> 3) * 2048 + ks1 * 256 + (rq & 15) * 16, 0);
    }
    BAR();
    STAMP(1);
    const int ab = r >> 4, g4 = (r >> 2) & 3, sp4 = r & 3;
    const int kvoff = ((ab * 4 + g4) * 4096 + 3072 + sp4 * 256 + 32 * w) * 256;
    Unit KU, VU;
    load_nat(KU, rsK, l * 16, kvoff, 1024);
    load_nat(VU, rsV, l * 16, kvoff, 1024);
    {
      unsigned char* ks = smem + L_KS + w * 8192;
#pragma unroll
      for (int i = 0; i < 8; ++i) {
        const int key = 4 * i + g;
        *(v4u*)(ks + key * 256 + ((m ^ (key & 15)) * 16)) = KU.a[i];
      }
      asm volatile("s_waitcnt lgkmcnt(0)" ::: "memory");
#pragma unroll
      for (int t = 0; t < 2; ++t) {
        const int key = 16 * t + 4 * (m & 3) + (m >> 2);
#pragma unroll
        for (int kk = 0; kk < 4; ++kk) KU.a[t * 4 + kk] = *(const v4u*)(ks + key * 256 + (((4 * kk + g) ^ (key & 15)) * 16));
      }
#pragma unroll
      for (int i = 0; i < 8; ++i) PIN(KU.a[i]);
    }
    transpose_unit(VU);
    STAMP(2);
    {
      const int qoff = OFF_Q + (ab * 4 + g4) * 16384 + w * 2048 + l * 16;
      v4u d0, d1;
      POLL((d0 = LDSC(rsW, qoff, 0), d1 = LDSC(rsW, qoff + 1024, 0)), bad4(d0, d1, T));
      STAMP(4);
      const v4f a = dec4acc(d1, dec4(d0));
      const u32 p = pack2(sadd32(a.x, a.y), sadd32(a.z, a.w));
      unsigned char* qp = smem + L_SA + w * RROW + (32 * (rq >> 3) + (rq & 7) + 8 * (l >> 5)) * 2;
      *(u16*)qp = (u16)p;
      *(u16*)(qp + 32) = (u16)(p >> 16);
      BAR();
      v4f s0 = v4f{0.f, 0.f, 0.f, 0.f}, s1 = v4f{0.f, 0.f, 0.f, 0.f};
#pragma unroll
      for (int kk = 0; kk < 4; ++kk) {
        const v4u qf = *(const v4u*)(smem + L_SA + (m & 7) * RROW + (32 * kk + 8 * g) * 2);
        s0 = mfma(KU.a[kk], qf, s0);
        s1 = mfma(KU.a[4 + kk], qf, s1);
      }
      float sc[8];
#pragma unroll
      for (int i = 0; i < 4; i += 2) {
        const u32 pa = pack2(s0[i], s0[i + 1]), pb = pack2(s1[i], s1[i + 1]);
        const u32 qa = pack2(bflo(pa) * 0.08838834764831845f, bfhi(pa) * 0.08838834764831845f);
        const u32 qb = pack2(bflo(pb) * 0.08838834764831845f, bfhi(pb) * 0.08838834764831845f);
        sc[i] = bflo(qa); sc[i + 1] = bfhi(qa); sc[4 + i] = bflo(qb); sc[5 + i] = bfhi(qb);
      }
      float mx = fmaxf(fmaxf(fmaxf(sc[0], sc[1]), sc[2]), fmaxf(fmaxf(sc[3], sc[4]), sc[5]));
      mx = fmaxf(fmaxf(mx, sc[6]), sc[7]);
      mx = max32(max16(mx));
      if (l < 8) *(float*)(smem + L_R2 + l * 32 + w * 4) = mx;
      BAR();
      const v4f m0 = *(const v4f*)(smem + L_R2 + (m & 7) * 32), m1 = *(const v4f*)(smem + L_R2 + (m & 7) * 32 + 16);
      const float M = fmaxf(fmaxf(fmaxf(fmaxf(m0.x, m0.y), m0.z), fmaxf(fmaxf(m0.w, m1.x), m1.y)), fmaxf(m1.z, m1.w));
      const float nM = -M * 1.44269504088896341f;
#pragma unroll
      for (int i = 0; i < 8; ++i) sc[i] = __builtin_amdgcn_exp2f(fmaf(sc[i], 1.44269504088896341f, nM));
      const v4u pf = v4u{pack2(sc[0], sc[1]), pack2(sc[2], sc[3]), pack2(sc[4], sc[5]), pack2(sc[6], sc[7])};
      v4f acc[8];
      mma_first(acc, VU, pf);
      const v4f ls = mfma(v4u{0x3F803F80u, 0x3F803F80u, 0x3F803F80u, 0x3F803F80u}, pf, v4f{0.f, 0.f, 0.f, 0.f});
      store_red(smem + L_RB + w * RBLK, acc, m, g);
      if (l < 8) *(float*)(smem + L_R2 + 512 + l * 32 + w * 4) = ls.x;
      BAR();
      if (w < 4) {
        const int hh = tid >> 5;
        v4f o = *(const v4f*)(smem + L_RB + hh * RROW + rq * 16);
#pragma unroll
        for (int k = 1; k < 8; ++k) o += *(const v4f*)(smem + L_RB + k * RBLK + hh * RROW + rq * 16);
        STSC(enc4(o, T), rsW, OFF_O + (((g4 * 8 + hh) * 8 + ab) * 4 + sp4) * 512 + rq * 16, 0);
      } else if (w == 4) {
        const v4f l0 = *(const v4f*)(smem + L_R2 + 512 + (m & 7) * 32), l1 = *(const v4f*)(smem + L_R2 + 512 + (m & 7) * 32 + 16);
        const float L = ((l0.x + l0.y) + (l0.z + l0.w)) + ((l1.x + l1.y) + (l1.z + l1.w));
        if (l < 8) STSC((v4u{asu(M), asu(L), T.a, T.b}), rsW, OFF_OS + (((g4 * 8 + l) * 8 + ab) * 4 + sp4) * 16, 0);
      }
      BAR();
    }
    STAMP(5);
    if (r < 112) {
      const int mat = r & 1, t5 = r >> 1, ns5 = t5 % 14, ks5 = t5 / 14;
      const rsrc_t rsG = __builtin_amdgcn_make_buffer_rsrc((void*)(mat ? wu : wg), 0, 1024 * 3584 * 2, 0x00020000);
      const int so = (ks5 * 256 + (w >> 1) * 64) * 7168 + ns5 * 512 + (w & 1) * 256;
      load_rm(KU, rsG, 2 * g * 7168 + m * 16, so, 7168);
      load_rm(VU, rsG, 2 * g * 7168 + m * 16, so + 32 * 7168, 7168);
      transpose_unit(KU);
      transpose_unit(VU);
      STAMP(6);
      const int gi = w * 256 + ks5 * 64 + l;
      v4u du, ds;
      POLL((du = LDSC(rsW, OFF_US + gi * 32, 0), ds = LDSC(rsW, OFF_US + gi * 32 + 16, 0)), bad2(du, T) | bad2(ds, T));
      float uu[4] = {bflo(du.x), bfhi(du.x), bflo(du.y), bfhi(du.y)}, sb[4] = {bflo(ds.x), bfhi(ds.x), bflo(ds.y), bfhi(ds.y)};
#pragma unroll
      for (int k = 0; k < 4; ++k) { PIN(uu[k]); PIN(sb[k]); }
      STAMP(7);
      const int soff = OFF_ST + (w * 16 + m) * 16;
      v4u d0, sv;
      POLL((sv = LDSC(rsW, soff, 0), d0 = LDSC(rsW, OFF_X1 + gi * 16, 0)), bad2(sv, T) | bad2(d0, T));
      STAMP(8);
      const float mu = sum16(asf(sv.x)) * (1.f / 1024.f);
      const float var = rbf(fmaxf(sum16(asf(sv.y)) * (1.f / 1024.f) - mu * mu, 0.f));
      const float mb = rbf(mu);
      const float rden = __builtin_amdgcn_rcpf(rbf(__builtin_amdgcn_sqrtf(rbf(var + 1e-5f))));
      u32 hp[2];
#pragma unroll
      for (int t = 0; t < 2; ++t) {
        const u32 xd = t ? d0.y : d0.x;
        const u32 a = pack2(bflo(xd) - mb, bfhi(xd) - mb);
        const u32 b = pack2(bflo(a) * rden, bfhi(a) * rden);
        const u32 e = pack2(bflo(b) * uu[2 * t], bfhi(b) * uu[2 * t + 1]);
        hp[t] = pack2(bflo(e) + sb[2 * t], bfhi(e) + sb[2 * t + 1]);
      }
      *(v2u*)(smem + L_SA + w * RROW + l * 8) = v2u{hp[0], hp[1]};
      BAR();
      const v4u a0 = *(const v4u*)(smem + L_SA + (m & 7) * RROW + (w >> 1) * 128 + g * 16);
      const v4u a1 = *(const v4u*)(smem + L_SA + (m & 7) * RROW + (w >> 1) * 128 + 64 + g * 16);
      v4f acc[8];
      mma_first(acc, KU, a0);
      mma_next(acc, VU, a1);
      store_red(smem + L_RA + w * RBLK, acc, m, g);
      BAR();
      v4f s = *(const v4f*)(smem + L_RA + rch * RBLK + rb * RROW + rq * 16);
#pragma unroll
      for (int k = 1; k < 4; ++k) s += *(const v4f*)(smem + L_RA + (k * 2 + rch) * RBLK + rb * RROW + rq * 16);
      STSC(enc4(s, T), rsW, OFF_GU + (((ns5 * 4 + rch * 2 + (rq >> 4)) * 8 + rb) * 8 + mat * 4 + ks5) * 256 + (rq & 15) * 16, 0);
    }
    STAMP(12);
  }

  {
    const int ob = u >> 5, c32 = u & 31;
    float gbv = 0.f, x1v = 0.f;
    if (w == 7) {
      v4u d0, d1;
      POLL((d0 = LDSC(rsW, OFF_GB + u * 1024 + l * 16, 0), d1 = LDSC(rsW, OFF_X1 + ob * 4096 + (c32 * 8 + (l & 7)) * 16, 0)),
           bad4(d0, T) | bad2(d1, T));
      float z = fold4(dec4(d0));
      z += dpp<0x128>(z);
      gbv = rbf(z);
      const u32 xd = g < 2 ? d1.x : d1.y;
      x1v = (g & 1) ? bfhi(xd) : bflo(xd);
    } else {
      v4u d0;
      POLL(d0 = LDSC(rsW, OFF_DN + u * 7168 + w * 1024 + l * 16, 0), bad4(d0, T));
      STAMP(13);
      float z = fold4(dec4(d0));
      z += dpp<0x128>(z);
      *(float*)(smem + L_R2 + 6144 + (g * 8 + (l & 7)) * 32 + w * 4) = z;
    }
    BAR();
    if (w == 7) {
      const v4f s0 = *(const v4f*)(smem + L_R2 + 6144 + (g * 8 + (l & 7)) * 32), s1 = *(const v4f*)(smem + L_R2 + 6144 + (g * 8 + (l & 7)) * 32 + 16);
      const float f = ((s0.x + s0.y) + (s0.z + s0.w)) + ((s1.x + s1.y) + s1.z);
      const float o = x1v + rbf(gbv * rbf(f));
      if ((l & 8) == 0) out[ob * 1024 + c32 * 32 + (l & 7) + 8 * prow] = (u16)(pack2(o, 0.f) & 0xFFFFu);
      if (u == 0 && l == 0) __builtin_amdgcn_raw_buffer_store_b64(v2u{elo, ehi}, rsW, 0, 0, 16);
    }
  }
  STAMP(14);
  if (HOLDC(c)) while (__builtin_amdgcn_s_memrealtime() - hold_t < 4000) __builtin_amdgcn_s_sleep(4);
}

extern "C" void kernel_launch(void* const* d_in, const int* in_sizes, int n_in, void* d_out, int out_size, void* d_ws,
                              size_t ws_size, hipStream_t stream) {
  fused_block<<<256, 512, 0, stream>>>((const u16*)d_in[0], (const u16*)d_in[1], (const u16*)d_in[2], (const u16*)d_in[3],
                                       (const u16*)d_in[4], (const u16*)d_in[6], (const u16*)d_in[7], (const u16*)d_in[8],
                                       (const u16*)d_in[9], (const u16*)d_in[10], (u16*)d_out, (unsigned char*)d_ws);
}
```

```cpp
#include <hip/hip_runtime.h>
#include <stdint.h>

typedef unsigned u32;
typedef unsigned long long u64;
typedef unsigned short u16;
typedef u32 v4u __attribute__((ext_vector_type(4)));
typedef u32 v2u __attribute__((ext_vector_type(2)));
typedef float v4f __attribute__((ext_vector_type(4)));
typedef float v2f __attribute__((ext_vector_type(2)));
typedef __bf16 v8b __attribute__((ext_vector_type(8)));
typedef __bf16 v2b __attribute__((ext_vector_type(2)));
typedef __amdgpu_buffer_rsrc_t rsrc_t;
#define DEVI static __device__ __forceinline__

constexpr int OFF_P1 = 0x001000;
constexpr int OFF_H = 0x081000;
constexpr int OFF_Q = 0x089000;
constexpr int OFF_O = 0x109000;
constexpr int OFF_OS = 0x189000;
constexpr int OFF_MOD = 0x18D000;
constexpr int OFF_GB = 0x24D000;
constexpr int OFF_WO = 0x28D000;
constexpr int OFF_X1 = 0x38D000;
constexpr int OFF_US = 0x395000;
constexpr int OFF_ST = 0x3A5000;
constexpr int OFF_GU = 0x3A6000;
constexpr int OFF_DN = 0x486000;
constexpr int WS_USED = 0x800000;

constexpr int RROW = 528;
constexpr int RBLK = 8 * RROW;
constexpr int L_KS = 0;
constexpr int L_RA = 65536;
constexpr int L_RB = L_RA + 8 * RBLK;
constexpr int L_SA = L_RB + 8 * RBLK;
constexpr int L_R2 = L_SA + RBLK;
constexpr int L_TOTAL = L_R2 + 8192;
constexpr unsigned SPIN_MAX = 200000;

#define LDNT(rs, vo, so) __builtin_amdgcn_raw_buffer_load_b128(rs, vo, so, 2)
#define LDPL(rs, vo, so) __builtin_amdgcn_raw_buffer_load_b128(rs, vo, so, 0)
#define LDSC(rs, vo, so) __builtin_amdgcn_raw_buffer_load_b128(rs, vo, so, 16)
#define STSC(v, rs, vo, so) __builtin_amdgcn_raw_buffer_store_b128(v, rs, vo, so, 16)
#define BAR() asm volatile("s_waitcnt lgkmcnt(0)\n\ts_barrier" ::: "memory")
#define FENCE() asm volatile("" ::: "memory")
#define PIN(x) asm volatile("" : "+v"(x))

DEVI float asf(u32 x) { return __uint_as_float(x); }
DEVI u32 asu(float x) { return __float_as_uint(x); }
DEVI float bflo(u32 x) { return asf(x << 16); }
DEVI float bfhi(u32 x) { return asf(x & 0xFFFF0000u); }
DEVI u32 pack2(float lo, float hi) {
  v2b p = {(__bf16)lo, (__bf16)hi};
  return __builtin_bit_cast(u32, p);
}
DEVI float rbf(float f) { return (float)(__bf16)f; }
DEVI float silu_div(float v) { return v / (1.f + __expf(-v)); }

template <int CTRL> DEVI float dpp(float x) { return asf(__builtin_amdgcn_update_dpp(0u, asu(x), CTRL, 0xf, 0xf, false)); }
template <int CTRL> DEVI u32 dppu(u32 x) { return __builtin_amdgcn_update_dpp(0u, x, CTRL, 0xf, 0xf, false); }
DEVI float sum16(float x) { x += dpp<0x128>(x); x += dpp<0x124>(x); x += dpp<0x122>(x); x += dpp<0x121>(x); return x; }
DEVI float sadd32(float a, float b) { v2u s = __builtin_amdgcn_permlane32_swap(asu(a), asu(b), false, false); return asf(s.x) + asf(s.y); }
DEVI float sadd16(float a, float b) { v2u s = __builtin_amdgcn_permlane16_swap(asu(a), asu(b), false, false); return asf(s.x) + asf(s.y); }
DEVI float max16(float x) { v2u a = __builtin_amdgcn_permlane16_swap(asu(x), asu(x), false, false); return fmaxf(asf(a.x), asf(a.y)); }
DEVI float max32(float x) { v2u a = __builtin_amdgcn_permlane32_swap(asu(x), asu(x), false, false); return fmaxf(asf(a.x), asf(a.y)); }
DEVI float sum64(float x) { x = sum16(x); x = sadd16(x, x); return sadd32(x, x); }
DEVI bool any_bad(u32 bad) { return __builtin_amdgcn_ballot_w64(bad != 0u) != 0ull; }

struct Tag { u32 t0, t1, t2, t3, e0, a, b; };
DEVI v4u enc4(v4f v, const Tag& T) {
  float mx = fmaxf(fmaxf(fabsf(v.x), fabsf(v.y)), fabsf(v.z));
  mx = fmaxf(fmaxf(mx, fabsf(v.w)), 0x1p-106f) * 1.00000095367431640625f;
  const u32 eb = asu(mx) & 0x7F800000u;
  const float inv = asf(0x89000000u - eb);
  const float mg = 12582912.f;
  return v4u{(asu(fmaf(v.x, inv, mg)) << 10) | ((eb >> 23) + T.e0), (asu(fmaf(v.y, inv, mg)) << 10) | T.t1,
             (asu(fmaf(v.z, inv, mg)) << 10) | T.t2, (asu(fmaf(v.w, inv, mg)) << 10) | T.t3};
}
DEVI u32 bad3(v4u g, const Tag& T) { return (g.y ^ T.t1) | (g.z ^ T.t2) | (g.w ^ T.t3); }
DEVI u32 badf(u32 y, u32 z) { return (y & 0x3FFu) | (z & 0x300u); }
DEVI u32 bad4(v4u g, const Tag& T) { return badf(bad3(g, T), g.x ^ T.t0); }
DEVI u32 bad4(v4u g, v4u h, const Tag& T) { return badf(bad3(g, T) | bad3(h, T), (g.x ^ T.t0) | (h.x ^ T.t0)); }
DEVI u32 bad2(v4u g, const Tag& T) { return (g.z ^ T.a) | (g.w ^ T.b); }
DEVI float gscale(v4u g) { return fabsf(asf(g.x << 23)); }
DEVI v4f dec4(v4u g) {
  const float s = gscale(g);
  return v4f{(float)((int)g.x >> 10) * s, (float)((int)g.y >> 10) * s, (float)((int)g.z >> 10) * s, (float)((int)g.w >> 10) * s};
}
DEVI v4f dec4acc(v4u g, v4f a) {
  const float s = gscale(g);
  return v4f{fmaf((float)((int)g.x >> 10), s, a.x), fmaf((float)((int)g.y >> 10), s, a.y), fmaf((float)((int)g.z >> 10), s, a.z),
             fmaf((float)((int)g.w >> 10), s, a.w)};
}
DEVI float fold4(v4f v) { return sadd16(sadd32(v.x, v.y), sadd32(v.z, v.w)); }

struct Unit { v4u a[8]; };
DEVI void load_nat(Unit& U, rsrc_t rs, int voff, int soff, int stride) {
#pragma unroll
  for (int j = 0; j < 8; ++j) U.a[j] = LDNT(rs, voff, soff + j * stride);
}
DEVI void load_rm(Unit& U, rsrc_t rs, int voff, int soff, int stride) {
#pragma unroll
  for (int j = 0; j < 8; ++j) U.a[j] = LDNT(rs, voff, soff + ((j >> 2) + 8 * (((j & 1) << 1) | ((j >> 1) & 1))) * stride);
}
DEVI void transpose_unit(Unit& U) {
  Unit T;
#pragma unroll
  for (int i = 0; i < 8; ++i) {
    const u32 sel = (i & 1) ? 0x07060302u : 0x05040100u;
#pragma unroll
    for (int p = 0; p < 4; ++p) T.a[i][p] = __builtin_amdgcn_perm(U.a[2 * p + 1][i >> 1], U.a[2 * p][i >> 1], sel);
  }
#pragma unroll
  for (int i = 0; i < 8; ++i) { U.a[i] = T.a[i]; PIN(U.a[i]); }
}
DEVI v4f mfma(v4u a, v4u b, v4f c) {
  return __builtin_amdgcn_mfma_f32_16x16x32_bf16(__builtin_bit_cast(v8b, a), __builtin_bit_cast(v8b, b), c, 0, 0, 0);
}
DEVI void mma_first(v4f (&acc)[8], const Unit& U, v4u act) {
#pragma unroll
  for (int i = 0; i < 8; ++i) acc[i] = mfma(U.a[i], act, v4f{0.f, 0.f, 0.f, 0.f});
}
DEVI void mma_next(v4f (&acc)[8], const Unit& U, v4u act) {
#pragma unroll
  for (int i = 0; i < 8; ++i) acc[i] = mfma(U.a[i], act, acc[i]);
}
DEVI void store_red(unsigned char* blk, const v4f (&acc)[8], int m, int g) {
  if (m < 8) {
#pragma unroll
    for (int i = 0; i < 8; ++i) *(v4f*)(blk + m * RROW + g * 128 + i * 16) = acc[i];
  }
}
DEVI v4u silu_frag(v2u c) {
  const u32 p0 = pack2(silu_div(bflo(c.x)), silu_div(bfhi(c.x))), p1 = pack2(silu_div(bflo(c.y)), silu_div(bfhi(c.y)));
  return v4u{p0, p1, dppu<0x128>(p0), dppu<0x128>(p1)};
}

#define POLL(loads, badexpr) \
  for (unsigned sp_ = 0; sp_ < SPIN_MAX; ++sp_) { FENCE(); loads; if (!any_bad(badexpr)) break; }

__global__ void __launch_bounds__(512, 2)
fused_block(const u16* __restrict__ x, const u16* __restrict__ kc, const u16* __restrict__ vc, const u16* __restrict__ wq,
            const u16* __restrict__ wo, const u16* __restrict__ wg, const u16* __restrict__ wu, const u16* __restrict__ wd,
            const u16* __restrict__ cc, const u16* __restrict__ wm, u16* __restrict__ out, unsigned char* __restrict__ ws) {
  __shared__ __attribute__((aligned(16))) unsigned char smem[L_TOTAL];
  const int tid = threadIdx.x, l = tid & 63, w = __builtin_amdgcn_readfirstlane(tid >> 6);
  const int m = l & 15, g = l >> 4;
  const int c = blockIdx.x, xcd = c & 7, jc = c >> 3, role = jc >> 4, r = xcd * 16 + (jc & 15), u = xcd * 32 + jc;
  const rsrc_t rsW = __builtin_amdgcn_make_buffer_rsrc((void*)ws, 0, WS_USED, 0x00020000);
  const rsrc_t rsM = __builtin_amdgcn_make_buffer_rsrc((void*)wm, 0, 1024 * 6144 * 2, 0x00020000);
  const rsrc_t rsC = __builtin_amdgcn_make_buffer_rsrc((void*)cc, 0, 8 * 1024 * 2, 0x00020000);
  asm volatile("" ::"s"(x), "s"(kc), "s"(vc), "s"(wq), "s"(wo), "s"(wg), "s"(wu), "s"(wd), "s"(cc), "s"(wm), "s"(out), "s"(ws));
  Tag T;
  u32 elo, ehi;
  auto make_tag = [&](v2u ev) {
    const u64 E = (((u64)ev.y << 32) | ev.x) + 1ull;
    elo = __builtin_amdgcn_readfirstlane((u32)E);
    ehi = __builtin_amdgcn_readfirstlane((u32)(E >> 32));
    T.t0 = (elo & 3u) << 8;
    T.t1 = (elo >> 2) & 0x3FFu;
    T.t2 = (elo >> 12) & 0x3FFu;
    T.t3 = elo >> 22;
    T.e0 = T.t0 - 20u;
    T.a = elo;
    T.b = ehi ^ 0x5A5A5A5Au;
  };
  const int rb = tid >> 6, rch = (tid >> 5) & 1, rq = tid & 31;
  const int prow = ((g & 1) << 1) | (g >> 1);

  if (role == 0) {
    const rsrc_t rsQ = __builtin_amdgcn_make_buffer_rsrc((void*)wq, 0, 1024 * 4096 * 2, 0x00020000);
    const rsrc_t rsO = __builtin_amdgcn_make_buffer_rsrc((void*)wo, 0, 4096 * 1024 * 2, 0x00020000);
    const rsrc_t rsX = __builtin_amdgcn_make_buffer_rsrc((void*)x, 0, w == 7 ? 8 * 1024 * 2 : 0, 0x00020000);
    const int hd = r & 31, ks2 = r >> 5;
    const int ob = (r & 15) >> 1, ocb = 2 * xcd + (r & 1), ro = ob * 16 + ocb;
    const int ocol = ob * 1024 + ocb * 64 + 32 * (m >> 3) + (m & 7);
    const v2u ev = __builtin_amdgcn_raw_buffer_load_b64(rsW, 0, 0, 16);
    const v4u xa = LDPL(rsX, ob * 2048 + l * 32, 0);
    const v4u xb = LDPL(rsX, ob * 2048 + l * 32 + 16, 0);
    const u32 xs0 = __builtin_amdgcn_raw_buffer_load_b16(rsX, (ocol + 8 * (g & 1)) * 2, 0, 0);
    const u32 xs1 = __builtin_amdgcn_raw_buffer_load_b16(rsX, (ocol + 8 * (g & 1) + 16) * 2, 0, 0);
    u32 xo = __builtin_amdgcn_raw_buffer_load_b16(rsX, (ocol + 8 * prow) * 2, 0, 0);
    Unit U;
    {
      const int vo = 2 * g * 8192 + m * 16, so = (ks2 * 256 + 32 * w) * 8192 + hd * 256;
      make_tag(ev);
      FENCE();
      load_rm(U, rsQ, vo, so, 8192);
    }
    if (w == 7) {
      float s = 0.f;
#pragma unroll
      for (int k = 0; k < 4; ++k) s += bflo(xa[k]) + bfhi(xa[k]) + bflo(xb[k]) + bfhi(xb[k]);
      const float mu = sum64(s) * (1.f / 1024.f);
      float v = 0.f;
#pragma unroll
      for (int k = 0; k < 4; ++k) {
        float d;
        d = bflo(xa[k]) - mu; v += d * d;
        d = bfhi(xa[k]) - mu; v += d * d;
        d = bflo(xb[k]) - mu; v += d * d;
        d = bfhi(xb[k]) - mu; v += d * d;
      }
      v = rbf(sum64(v) * (1.f / 1024.f));
      const float mean1 = rbf(mu), den1 = rbf(sqrtf(rbf(v + 1e-5f)));
      float xn0 = rbf(rbf(bflo(xs0) - mean1) / den1), xn1 = rbf(rbf(bflo(xs1) - mean1) / den1);
      PIN(xn0); PIN(xn1);
      const int poff = OFF_P1 + ro * 4096 + g * 512 + m * 16;
      v4u d0, d1, d2, d3;
      POLL((d0 = LDSC(rsW, poff, 0), d1 = LDSC(rsW, poff + 256, 0), d2 = LDSC(rsW, poff + 2048, 0), d3 = LDSC(rsW, poff + 2304, 0)),
           bad4(d0, d1, T) | bad4(d2, d3, T));
      const v4f sa = dec4acc(d1, dec4(d0)), ca = dec4acc(d3, dec4(d2));
      const u32 p = pack2(sadd16(sadd32(sa.x, ca.x), sadd32(sa.y, ca.y)), sadd16(sadd32(sa.z, ca.z), sadd32(sa.w, ca.w)));
      const v2u sc = __builtin_amdgcn_permlane32_swap(p, p, false, false);
      const float h0 = rbf(xn0 * rbf(1.f + bflo(sc.y))) + bflo(sc.x), h1 = rbf(xn1 * rbf(1.f + bfhi(sc.y))) + bfhi(sc.x);
      const u32 ph = pack2(h0, h1);
      const v2u hp = __builtin_amdgcn_permlane16_swap(ph, ph, false, false);
      if (l < 16) STSC((v4u{hp.x, hp.y, T.a, T.b}), rsW, OFF_H + ob * 4096 + (ocb * 16 + m) * 16, 0);
    }
    transpose_unit(U);
    {
      const int hoff = OFF_H + (m & 7) * 4096 + ((ks2 * 8 + w) * 8 + 2 * g + (m >> 3)) * 16;
      v4u hg;
      POLL(hg = LDSC(rsW, hoff, 0), bad2(hg, T));
      v4f acc[8];
      mma_first(acc, U, v4u{hg.x, hg.y, dppu<0x128>(hg.x), dppu<0x128>(hg.y)});
      store_red(smem + L_RA + w * RBLK, acc, m, g);
      BAR();
      if (tid < 256) {
        const int b = tid >> 5;
        v4f s = *(const v4f*)(smem + L_RA + b * RROW + rq * 16);
#pragma unroll
        for (int k = 1; k < 8; ++k) s += *(const v4f*)(smem + L_RA + k * RBLK + b * RROW + rq * 16);
        STSC(enc4(s, T), rsW, OFF_Q + (b * 4 + (hd >> 3)) * 16384 + (hd & 7) * 2048 + ks2 * 512 + rq * 16, 0);
      }
      BAR();
    }
    const int hd4 = r >> 2, nq = r & 3;
    Unit UO;
    {
      const int ksm = r >> 4, nsm = r & 15;
      const v2u c8 = __builtin_amdgcn_raw_buffer_load_b64(rsC, (m & 7) * 2048 + (ksm * 128 + (w >> 1) * 32 + 8 * g + 4 * (m >> 3)) * 2, 0, 0);
      load_nat(U, rsM, 8 * g * 12288 + m * 16, (ksm * 128 + (w >> 1) * 32) * 12288 + 4096 + nsm * 512 + (w & 1) * 256, 12288);
      load_rm(UO, rsO, 2 * g * 2048 + m * 16, (hd4 * 128 + (w >> 1) * 32) * 2048 + nq * 512 + (w & 1) * 256, 2048);
      const v4u cf = silu_frag(c8);
      transpose_unit(U);
      v4f acc[8];
      mma_first(acc, U, cf);
      store_red(smem + L_RB + w * RBLK, acc, m, g);
      BAR();
      v4f s = *(const v4f*)(smem + L_RB + rch * RBLK + rb * RROW + rq * 16);
#pragma unroll
      for (int k = 1; k < 4; ++k) s += *(const v4f*)(smem + L_RB + (k * 2 + rch) * RBLK + rb * RROW + rq * 16);
      const int which = nsm >> 2;
      const int off = which < 3 ? OFF_MOD + (rb * 16 + (nsm & 3) * 4 + rch * 2 + (rq >> 4)) * 6144 + which * 2048 + ksm * 256 + (rq & 15) * 16
                                : OFF_GB + (rb * 32 + (nsm & 3) * 8 + rch * 4 + (rq >> 3)) * 1024 + ksm * 128 + (rq & 7) * 16;
      STSC(enc4(s, T), rsW, off, 0);
    }
    transpose_unit(UO);
    {
      const int ooff = OFF_O + (hd4 * 8 + w) * 2048 + l * 16;
      const int soff = OFF_OS + ((hd4 * 8 + w) * 4 + (l & 3)) * 16;
      v4u d0, d1, sv;
      POLL((sv = LDSC(rsW, soff, 0), d0 = LDSC(rsW, ooff, 0), d1 = LDSC(rsW, ooff + 1024, 0)), bad2(sv, T) | bad4(d0, d1, T));
      const float Ms = asf(sv.x), Ls = asf(sv.y);
      float M = fmaxf(Ms, dpp<0xB1>(Ms));
      M = fmaxf(M, dpp<0x4E>(M));
      const float es = __expf(Ms - M);
      float den = es * Ls;
      den += dpp<0xB1>(den);
      den += dpp<0x4E>(den);
      const float fs = es * __builtin_amdgcn_rcpf(den);
      const float f0 = dpp<0x00>(fs), f1 = dpp<0x55>(fs), f2 = dpp<0xAA>(fs), f3 = dpp<0xFF>(fs);
      const float fa = (l < 32 ? f0 : f1) * gscale(d0), fb = (l < 32 ? f2 : f3) * gscale(d1);
      float o4[4];
#pragma unroll
      for (int k = 0; k < 4; ++k) o4[k] = fmaf((float)((int)d1[k] >> 10), fb, (float)((int)d0[k] >> 10) * fa);
      *(u32*)(smem + L_SA + w * RROW + rq * 8 + (l >> 5) * 4) = pack2(sadd32(o4[0], o4[1]), sadd32(o4[2], o4[3]));
      BAR();
      const v4u a = *(const v4u*)(smem + L_SA + (m & 7) * RROW + (w >> 1) * 64 + g * 16);
      v4f acc[8];
      mma_first(acc, UO, a);
      store_red(smem + L_RA + w * RBLK, acc, m, g);
      BAR();
      v4f s = *(const v4f*)(smem + L_RA + rch * RBLK + rb * RROW + rq * 16);
#pragma unroll
      for (int k = 1; k < 4; ++k) s += *(const v4f*)(smem + L_RA + (k * 2 + rch) * RBLK + rb * RROW + rq * 16);
      STSC(enc4(s, T), rsW, OFF_WO + (rb * 16 + nq * 4 + rch * 2 + (rq >> 4)) * 8192 + hd4 * 256 + (rq & 15) * 16, 0);
    }
    float ga = 0.f;
    {
      v4u d;
      POLL(d = LDSC(rsW, OFF_MOD + ro * 6144 + (w < 6 ? w : 0) * 1024 + l * 16, 0), bad4(d, T));
      *(float*)(smem + L_R2 + l * 48 + w * 4) = fold4(dec4(d));
      BAR();
      if (w == 7) {
        const v4f s0 = *(const v4f*)(smem + L_R2 + l * 48);
        const v2f s1 = *(const v2f*)(smem + L_R2 + l * 48 + 16);
        ga = rbf(s0.x + s0.y);
        const u32 pu = pack2(1.f + rbf(s1.x + s1.y), s0.z + s0.w);
        const v2u a = __builtin_amdgcn_permlane16_swap(pu, pu, false, false);
        const u32 lu = __builtin_amdgcn_perm(a.y, a.x, 0x05040100u), ls = __builtin_amdgcn_perm(a.y, a.x, 0x07060302u);
        const v2u b = __builtin_amdgcn_permlane32_swap(lu, ls, false, false);
        if (g == 0 || g == 2)
          STSC((v4u{b.x, b.y, T.a, T.b}), rsW, OFF_US + ((ob * 256 + ocb * 16 + m) * 2 + (g >> 1)) * 16, 0);
      }
    }
    {
      v4u d;
      POLL(d = LDSC(rsW, OFF_WO + ro * 8192 + w * 1024 + l * 16, 0), bad4(d, T));
      *(float*)(smem + L_R2 + 3072 + l * 48 + w * 4) = fold4(dec4(d));
      BAR();
      if (w == 7) {
        const v4f s0 = *(const v4f*)(smem + L_R2 + 3072 + l * 48), s1 = *(const v4f*)(smem + L_R2 + 3072 + l * 48 + 16);
        const float attn = ((s0.x + s0.y) + (s0.z + s0.w)) + ((s1.x + s1.y) + (s1.z + s1.w));
        PIN(xo);
        const float x1 = rbf(bflo(xo) + rbf(ga * rbf(attn)));
        const u32 pz = asu(x1) >> 16;
        const v2u a = __builtin_amdgcn_permlane16_swap(pz, pz, false, false);
        const u32 lo = a.x | (a.y << 16);
        const v2u p = __builtin_amdgcn_permlane32_swap(lo, lo, false, false);
        if (l < 16) STSC((v4u{p.x, p.y, T.a, T.b}), rsW, OFF_X1 + ob * 4096 + (ocb * 16 + m) * 16, 0);
        const float gs = sum64(x1);
        const float gd = x1 - gs * (1.f / 64.f);
        const float gm2 = sum64(gd * gd);
        if (l == 0) STSC((v4u{asu(gs), asu(gm2), T.a, T.b}), rsW, OFF_ST + ro * 16, 0);
      }
      BAR();
    }
    if (r < 112) {
      const rsrc_t rsD = __builtin_amdgcn_make_buffer_rsrc((void*)wd, 0, 3584 * 1024 * 2, 0x00020000);
      const int k6 = r >> 1, nh = r & 1;
      load_rm(U, rsD, 2 * g * 2048 + m * 16, (k6 * 64 + (w & 1) * 32) * 2048 + nh * 1024 + (w >> 1) * 256, 2048);
      transpose_unit(U);
      const int goff = OFF_GU + (k6 * 8 + w) * 2048 + l * 16;
      v4u d0, d1;
      POLL((d0 = LDSC(rsW, goff, 0), d1 = LDSC(rsW, goff + 1024, 0)), bad4(d0, d1, T));
      const v4f gt = dec4(d0), up = dec4(d1);
      const u32 p = pack2(sadd16(sadd32(gt.x, up.x), sadd32(gt.y, up.y)), sadd16(sadd32(gt.z, up.z), sadd32(gt.w, up.w)));
      const v2u s = __builtin_amdgcn_permlane32_swap(p, p, false, false);
      const float g0 = bflo(s.x), g1 = bfhi(s.x);
      const float a0 = rbf(g0 * __builtin_amdgcn_rcpf(1.f + __expf(-g0))) * bflo(s.y);
      const float a1 = rbf(g1 * __builtin_amdgcn_rcpf(1.f + __expf(-g1))) * bfhi(s.y);
      if (l < 32) *(u32*)(smem + L_SA + w * RROW + m * 8 + g * 4) = pack2(a0, a1);
      BAR();
      const v4u a = *(const v4u*)(smem + L_SA + (m & 7) * RROW + (w & 1) * 64 + g * 16);
      v4f acc[8];
      mma_first(acc, U, a);
      store_red(smem + L_RB + w * RBLK, acc, m, g);
      BAR();
#pragma unroll
      for (int hh = 0; hh < 2; ++hh) {
        const int cq = hh * 2 + rch;
        const v4f s2 = *(const v4f*)(smem + L_RB + (2 * cq) * RBLK + rb * RROW + rq * 16) +
                       *(const v4f*)(smem + L_RB + (2 * cq + 1) * RBLK + rb * RROW + rq * 16);
        STSC(enc4(s2, T), rsW, OFF_DN + (rb * 32 + nh * 16 + cq * 4 + (rq >> 3)) * 7168 + k6 * 128 + (rq & 7) * 16, 0);
      }
    }
  } else {
    const rsrc_t rsK = __builtin_amdgcn_make_buffer_rsrc((void*)kc, 0, 8 * 4 * 4096 * 128 * 2, 0x00020000);
    const rsrc_t rsV = __builtin_amdgcn_make_buffer_rsrc((void*)vc, 0, 8 * 4 * 4096 * 128 * 2, 0x00020000);
    const v2u ev = __builtin_amdgcn_raw_buffer_load_b64(rsW, 0, 0, 16);
    const int ks1 = r & 7, ns1 = xcd + (r & 8);
    if (w < 4) {
      Unit PU;
      const v2u c8 = __builtin_amdgcn_raw_buffer_load_b64(rsC, (m & 7) * 2048 + (ks1 * 128 + w * 32 + 8 * g + 4 * (m >> 3)) * 2, 0, 0);
      load_nat(PU, rsM, 8 * g * 12288 + m * 16, (ks1 * 128 + w * 32) * 12288 + ns1 * 256, 12288);
      const v4u cf = silu_frag(c8);
      transpose_unit(PU);
      v4f acc[8];
      mma_first(acc, PU, cf);
      store_red(smem + L_RA + w * RBLK, acc, m, g);
    }
    make_tag(ev);
    const int ab = r >> 4, g4 = (r >> 2) & 3, sp4 = r & 3;
    const int kvoff = ((ab * 4 + g4) * 4096 + 3072 + sp4 * 256 + 32 * w) * 256;
    Unit KU, VU;
    BAR();
    if (w < 4) {
      const int b = tid >> 5;
      v4f s = *(const v4f*)(smem + L_RA + b * RROW + rq * 16);
#pragma unroll
      for (int k = 1; k < 4; ++k) s += *(const v4f*)(smem + L_RA + k * RBLK + b * RROW + rq * 16);
      STSC(enc4(s, T), rsW, OFF_P1 + (b * 16 + (ns1 & 7) * 2 + (rq >> 4)) * 4096 + (ns1 >> 3) * 2048 + ks1 * 256 + (rq & 15) * 16, 0);
    }
    BAR();
    load_nat(KU, rsK, l * 16, kvoff, 1024);
    load_nat(VU, rsV, l * 16, kvoff, 1024);
    {
      unsigned char* ks = smem + L_KS + w * 8192;
#pragma unroll
      for (int i = 0; i < 8; ++i) {
        const int key = 4 * i + g;
        *(v4u*)(ks + key * 256 + ((m ^ (key & 15)) * 16)) = KU.a[i];
      }
      asm volatile("s_waitcnt lgkmcnt(0)" ::: "memory");
#pragma unroll
      for (int t = 0; t < 2; ++t) {
        const int key = 16 * t + 4 * (m & 3) + (m >> 2);
#pragma unroll
        for (int kk = 0; kk < 4; ++kk) KU.a[t * 4 + kk] = *(const v4u*)(ks + key * 256 + (((4 * kk + g) ^ (key & 15)) * 16));
      }
#pragma unroll
      for (int i = 0; i < 8; ++i) PIN(KU.a[i]);
    }
    transpose_unit(VU);
    {
      const int qoff = OFF_Q + (ab * 4 + g4) * 16384 + w * 2048 + l * 16;
      v4u d0, d1;
      POLL((d0 = LDSC(rsW, qoff, 0), d1 = LDSC(rsW, qoff + 1024, 0)), bad4(d0, d1, T));
      const v4f a = dec4acc(d1, dec4(d0));
      const u32 p = pack2(sadd32(a.x, a.y), sadd32(a.z, a.w));
      unsigned char* qp = smem + L_SA + w * RROW + (32 * (rq >> 3) + (rq & 7) + 8 * (l >> 5)) * 2;
      *(u16*)qp = (u16)p;
      *(u16*)(qp + 32) = (u16)(p >> 16);
      BAR();
      v4f s0 = v4f{0.f, 0.f, 0.f, 0.f}, s1 = v4f{0.f, 0.f, 0.f, 0.f};
#pragma unroll
      for (int kk = 0; kk < 4; ++kk) {
        const v4u qf = *(const v4u*)(smem + L_SA + (m & 7) * RROW + (32 * kk + 8 * g) * 2);
        s0 = mfma(KU.a[kk], qf, s0);
        s1 = mfma(KU.a[4 + kk], qf, s1);
      }
      float sc[8];
#pragma unroll
      for (int i = 0; i < 4; i += 2) {
        const u32 pa = pack2(s0[i], s0[i + 1]), pb = pack2(s1[i], s1[i + 1]);
        const u32 qa = pack2(bflo(pa) * 0.08838834764831845f, bfhi(pa) * 0.08838834764831845f);
        const u32 qb = pack2(bflo(pb) * 0.08838834764831845f, bfhi(pb) * 0.08838834764831845f);
        sc[i] = bflo(qa); sc[i + 1] = bfhi(qa); sc[4 + i] = bflo(qb); sc[5 + i] = bfhi(qb);
      }
      float mx = fmaxf(fmaxf(fmaxf(sc[0], sc[1]), sc[2]), fmaxf(fmaxf(sc[3], sc[4]), sc[5]));
      mx = fmaxf(fmaxf(mx, sc[6]), sc[7]);
      mx = max32(max16(mx));
      if (l < 8) *(float*)(smem + L_R2 + l * 32 + w * 4) = mx;
      BAR();
      const v4f m0 = *(const v4f*)(smem + L_R2 + (m & 7) * 32), m1 = *(const v4f*)(smem + L_R2 + (m & 7) * 32 + 16);
      const float M = fmaxf(fmaxf(fmaxf(fmaxf(m0.x, m0.y), m0.z), fmaxf(fmaxf(m0.w, m1.x), m1.y)), fmaxf(m1.z, m1.w));
      const float nM = -M * 1.44269504088896341f;
#pragma unroll
      for (int i = 0; i < 8; ++i) sc[i] = __builtin_amdgcn_exp2f(fmaf(sc[i], 1.44269504088896341f, nM));
      const v4u pf = v4u{pack2(sc[0], sc[1]), pack2(sc[2], sc[3]), pack2(sc[4], sc[5]), pack2(sc[6], sc[7])};
      v4f acc[8];
      mma_first(acc, VU, pf);
      const v4f ls = mfma(v4u{0x3F803F80u, 0x3F803F80u, 0x3F803F80u, 0x3F803F80u}, pf, v4f{0.f, 0.f, 0.f, 0.f});
      store_red(smem + L_RB + w * RBLK, acc, m, g);
      if (l < 8) *(float*)(smem + L_R2 + 512 + l * 32 + w * 4) = ls.x;
      BAR();
      if (w < 4) {
        const int hh = tid >> 5;
        v4f o = *(const v4f*)(smem + L_RB + hh * RROW + rq * 16);
#pragma unroll
        for (int k = 1; k < 8; ++k) o += *(const v4f*)(smem + L_RB + k * RBLK + hh * RROW + rq * 16);
        STSC(enc4(o, T), rsW, OFF_O + (((g4 * 8 + hh) * 8 + ab) * 4 + sp4) * 512 + rq * 16, 0);
      } else if (w == 4) {
        const v4f l0 = *(const v4f*)(smem + L_R2 + 512 + (m & 7) * 32), l1 = *(const v4f*)(smem + L_R2 + 512 + (m & 7) * 32 + 16);
        const float L = ((l0.x + l0.y) + (l0.z + l0.w)) + ((l1.x + l1.y) + (l1.z + l1.w));
        if (l < 8) STSC((v4u{asu(M), asu(L), T.a, T.b}), rsW, OFF_OS + (((g4 * 8 + l) * 8 + ab) * 4 + sp4) * 16, 0);
      }
      BAR();
    }
    if (r < 112) {
      const int mat = r & 1, ns5 = r >> 3, ks5 = (r >> 1) & 3;
      const rsrc_t rsG = __builtin_amdgcn_make_buffer_rsrc((void*)(mat ? wu : wg), 0, 1024 * 3584 * 2, 0x00020000);
      const int so = (ks5 * 256 + (w >> 1) * 64) * 7168 + ns5 * 512 + (w & 1) * 256;
      load_rm(KU, rsG, 2 * g * 7168 + m * 16, so, 7168);
      load_rm(VU, rsG, 2 * g * 7168 + m * 16, so + 32 * 7168, 7168);
      transpose_unit(KU);
      transpose_unit(VU);
      const int gi = w * 256 + ks5 * 64 + l;
      v4u du, ds;
      POLL((du = LDSC(rsW, OFF_US + gi * 32, 0), ds = LDSC(rsW, OFF_US + gi * 32 + 16, 0)), bad2(du, T) | bad2(ds, T));
      float uu[4] = {bflo(du.x), bfhi(du.x), bflo(du.y), bfhi(du.y)}, sb[4] = {bflo(ds.x), bfhi(ds.x), bflo(ds.y), bfhi(ds.y)};
#pragma unroll
      for (int k = 0; k < 4; ++k) { PIN(uu[k]); PIN(sb[k]); }
      const int soff = OFF_ST + (w * 16 + m) * 16;
      v4u d0, sv;
      POLL((sv = LDSC(rsW, soff, 0), d0 = LDSC(rsW, OFF_X1 + gi * 16, 0)), bad2(sv, T) | bad2(d0, T));
      const float mu = sum16(asf(sv.x)) * (1.f / 1024.f);
      const float dm = asf(sv.x) * (1.f / 64.f) - mu;
      const float var = rbf(sum16(fmaf(64.f * dm, dm, asf(sv.y))) * (1.f / 1024.f));
      const float mb = rbf(mu);
      const float rden = __builtin_amdgcn_rcpf(rbf(__builtin_amdgcn_sqrtf(rbf(var + 1e-5f))));
      u32 hp[2];
#pragma unroll
      for (int t = 0; t < 2; ++t) {
        const u32 xd = t ? d0.y : d0.x;
        const u32 a = pack2(bflo(xd) - mb, bfhi(xd) - mb);
        const u32 b = pack2(bflo(a) * rden, bfhi(a) * rden);
        const u32 e = pack2(bflo(b) * uu[2 * t], bfhi(b) * uu[2 * t + 1]);
        hp[t] = pack2(bflo(e) + sb[2 * t], bfhi(e) + sb[2 * t + 1]);
      }
      *(v2u*)(smem + L_SA + w * RROW + l * 8) = v2u{hp[0], hp[1]};
      BAR();
      const v4u a0 = *(const v4u*)(smem + L_SA + (m & 7) * RROW + (w >> 1) * 128 + g * 16);
      const v4u a1 = *(const v4u*)(smem + L_SA + (m & 7) * RROW + (w >> 1) * 128 + 64 + g * 16);
      v4f acc[8];
      mma_first(acc, KU, a0);
      mma_next(acc, VU, a1);
      store_red(smem + L_RA + w * RBLK, acc, m, g);
      BAR();
      v4f s = *(const v4f*)(smem + L_RA + rch * RBLK + rb * RROW + rq * 16);
#pragma unroll
      for (int k = 1; k < 4; ++k) s += *(const v4f*)(smem + L_RA + (k * 2 + rch) * RBLK + rb * RROW + rq * 16);
      STSC(enc4(s, T), rsW, OFF_GU + (((ns5 * 4 + rch * 2 + (rq >> 4)) * 8 + rb) * 8 + mat * 4 + ks5) * 256 + (rq & 15) * 16, 0);
    }
  }

  {
    const int ob = u >> 5, c32 = u & 31;
    float gbv = 0.f, x1v = 0.f;
    if (w == 7) {
      v4u d0, d1;
      POLL((d0 = LDSC(rsW, OFF_GB + u * 1024 + l * 16, 0), d1 = LDSC(rsW, OFF_X1 + ob * 4096 + (c32 * 8 + (l & 7)) * 16, 0)),
           bad4(d0, T) | bad2(d1, T));
      float z = fold4(dec4(d0));
      z += dpp<0x128>(z);
      gbv = rbf(z);
      const u32 xd = g < 2 ? d1.x : d1.y;
      x1v = (g & 1) ? bfhi(xd) : bflo(xd);
    } else {
      v4u d0;
      POLL(d0 = LDSC(rsW, OFF_DN + u * 7168 + w * 1024 + l * 16, 0), bad4(d0, T));
      float z = fold4(dec4(d0));
      z += dpp<0x128>(z);
      *(float*)(smem + L_R2 + 6144 + (g * 8 + (l & 7)) * 32 + w * 4) = z;
    }
    BAR();
    if (w == 7) {
      const v4f s0 = *(const v4f*)(smem + L_R2 + 6144 + (g * 8 + (l & 7)) * 32), s1 = *(const v4f*)(smem + L_R2 + 6144 + (g * 8 + (l & 7)) * 32 + 16);
      const float f = ((s0.x + s0.y) + (s0.z + s0.w)) + ((s1.x + s1.y) + s1.z);
      const float o = x1v + rbf(gbv * rbf(f));
      if ((l & 8) == 0) out[ob * 1024 + c32 * 32 + (l & 7) + 8 * prow] = (u16)(pack2(o, 0.f) & 0xFFFFu);
      if (u == 0 && l == 0) __builtin_amdgcn_raw_buffer_store_b64(v2u{elo, ehi}, rsW, 0, 0, 16);
    }
  }
}

extern "C" void kernel_launch(void* const* d_in, const int* in_sizes, int n_in, void* d_out, int out_size, void* d_ws,
                              size_t ws_size, hipStream_t stream) {
  fused_block<<<256, 512, 0, stream>>>((const u16*)d_in[0], (const u16*)d_in[1], (const u16*)d_in[2], (const u16*)d_in[3],
                                       (const u16*)d_in[4], (const u16*)d_in[6], (const u16*)d_in[7], (const u16*)d_in[8],
                                       (const u16*)d_in[9], (const u16*)d_in[10], (u16*)d_out, (unsigned char*)d_ws);
}
```
